```python
import torch

def _softmax(x, axis=-1):
    return torch.softmax(x, dim=axis)
def _gelu(x):
    return torch.nn.functional.gelu(x, approximate="tanh")
def _silu(x):
    return torch.nn.functional.silu(x)
def _sigmoid(x):
    return torch.sigmoid(x)
def _layernorm(x, axis=-1, eps=1e-5):
    m = x.mean(dim=axis, keepdim=True)
    v = x.var(dim=axis, keepdim=True, unbiased=False)
    return (x - m) / torch.sqrt(v + eps)
def _scatter_add(tgt, idx, src, axis=0):
    return tgt.clone().index_add_(axis, idx, src)
def _segment_sum(src, seg, n):
    out = torch.zeros((n,) + src.shape[1:], dtype=src.dtype)
    return out.index_add_(0, seg, src)
def _pad(x, pw):
    flat = tuple(v for pair in reversed(pw) for v in pair)
    return torch.nn.functional.pad(x, flat)

kernel_name = 'gated_mix__linear_attn_block__ssm_conv_s4d__prompt'

def setup_inputs():

    _w = (lambda a: (lambda t: t.to(torch.bfloat16) if t.is_floating_point() else t)(torch.as_tensor(a)))
    def _A_setup():
        B, S, D = 1, 2048, 1024
        return {
            "x": _w((lambda *s: torch.randn(*s, dtype=torch.bfloat16) * 0.05)(B, S, D)),
            "wqkv": _w((lambda *s: torch.randn(*s, dtype=torch.bfloat16) * 0.8)(D, 3 * D)),
            "wo": _w((lambda *s: torch.randn(*s, dtype=torch.bfloat16) * 0.05)(D, D)),
        }
    _A_in = _A_setup()

    def _B_setup():
        Bsz, L, D, N = 1, 2048, 1024, 128
        return {
            "u": _w((lambda *s: torch.randn(*s, dtype=torch.bfloat16) * 0.05)(Bsz, L, D)),
            "log_A_real": _w((lambda *s: torch.randn(*s, dtype=torch.bfloat16) * 3.2)(D, N) * 0.5),
            "A_imag": _w((lambda *s: torch.randn(*s, dtype=torch.bfloat16) * 0.05)(D, N) * 3.14),
            "C_real": _w((lambda *s: torch.randn(*s, dtype=torch.bfloat16) * 0.8)(D, N) / N ** 0.5),
            "C_imag": _w((lambda *s: torch.randn(*s, dtype=torch.bfloat16) * 0.8)(D, N) / N ** 0.5),
            "log_dt": _w((lambda *s: torch.randn(*s, dtype=torch.bfloat16) * 0.05)(D) * 0.5 - 4.0),
            "D_skip": _w((lambda *s: torch.randn(*s, dtype=torch.bfloat16) * 0.1)(D)),
        }
    _B_in = _B_setup()

    return {
        "A_x": _A_in["x"],
        "A_wqkv": _A_in["wqkv"],
        "A_wo": _A_in["wo"],
        "B_u": _B_in["u"],
        "B_log_A_real": _B_in["log_A_real"],
        "B_A_imag": _B_in["A_imag"],
        "B_C_real": _B_in["C_real"],
        "B_C_imag": _B_in["C_imag"],
        "B_log_dt": _B_in["log_dt"],
        "B_D_skip": _B_in["D_skip"],
        "gate_w": _w((lambda *s: torch.randn(*s, dtype=torch.bfloat16) * 0.05)(1024) * 16.0),
    }

def reference(A_x, A_wqkv, A_wo, B_u, B_log_A_real, B_A_imag, B_C_real, B_C_imag, B_log_dt, B_D_skip, gate_w):
    def _A_ref(x, wqkv, wo):
        B, S, D = x.shape
        H = 32; Dh = D // H
        qkv = (lambda x, *d: x.permute(*d))((x @ wqkv).reshape(B, S, 3, H, Dh), 2, 0, 3, 1, 4)
        q, k, v = qkv[0], qkv[1], qkv[2]
        phi = lambda t: torch.where(t > 0, t + 1, torch.exp(t)) + 1e-6
        pq, pk = phi(q), phi(k)
        kv = torch.einsum("bhsd,bhse->bhde", pk, v)
        z = torch.einsum("bhsd->bhd", pk)
        num = torch.einsum("bhqd,bhde->bhqe", pq, kv)
        den = torch.einsum("bhqd,bhd->bhq", pq, z)[..., None] + 1e-6
        o = num / den
        return (lambda x, *d: x.permute(*d))(o, 0, 2, 1, 3).reshape(B, S, D) @ wo

    def _B_ref(u, log_A_real, A_imag, C_real, C_imag, log_dt, D_skip):
        L = u.shape[1]
        dt = torch.exp(log_dt)
        A = -torch.exp(log_A_real) + 1j * A_imag
        C = (C_real + 1j * C_imag) * dt[:, None]
        pos = (lambda x, dt: x.to(dt))(torch.arange(L), log_dt.dtype)
        P = torch.exp((A * dt[:, None])[:, :, None] * pos[None, None, :])
        K = 2.0 * torch.einsum("dn,dnl->dl", C, P).real
        L2 = 2 * L
        u_t = (lambda x, *d: x.permute(*d))(u, 0, 2, 1)
        Kf = (lambda x, *a, **k: torch.fft.rfft(x.float(), *a, **k))(K, L2)
        Uf = (lambda x, *a, **k: torch.fft.rfft(x.float(), *a, **k))(u_t, L2)
        y = (lambda x, *a, **k: torch.fft.irfft(x, *a, **k).to(torch.bfloat16))(Uf * Kf[None, :, :], L2)[:, :, :L]
        y = (lambda x, *d: x.permute(*d))(y, 0, 2, 1)
        return y + u * D_skip[None, None, :]

    x = A_x
    g = _sigmoid((x * gate_w).sum(-1, keepdims=True))
    a = _A_ref(x, A_wqkv, A_wo)
    b = _B_ref(x, B_log_A_real, B_A_imag, B_C_real, B_C_imag, B_log_dt, B_D_skip)
    return g * a + (1 - g) * b
```

```cpp
#include <hip/hip_runtime.h>
#include <hip/hip_bf16.h>
#include <stdint.h>
#define ROUND_POS 1
typedef unsigned short u16;
__device__ __forceinline__ float bf2f(u16 v){ return __uint_as_float(((unsigned)v)<<16); }
__device__ __forceinline__ u16 f2bf(float f){ unsigned u=__float_as_uint(f); u += 0x7FFF + ((u>>16)&1); return (u16)(u>>16); }
__device__ __forceinline__ float rbf(float f){ return bf2f(f2bf(f)); }
constexpr int S=2048, D=1024, H=32, DH=32, NM=128;

__global__ void gemm_naive(const u16* A, const u16* B, u16* C, int M, int N, int K){
  __shared__ float As[16][17], Bs[16][17];
  int tx=threadIdx.x, ty=threadIdx.y; int row=blockIdx.y*16+ty, col=blockIdx.x*16+tx;
  float acc=0;
  for(int k0=0;k0<K;k0+=16){
    As[ty][tx]=bf2f(A[(long)row*K+k0+tx]); Bs[ty][tx]=bf2f(B[(long)(k0+ty)*N+col]);
    __syncthreads();
    for(int k=0;k<16;k++) acc+=As[ty][k]*Bs[k][tx];
    __syncthreads();
  }
  C[(long)row*N+col]=f2bf(acc);
}
__device__ __forceinline__ float phi(float t){ float r = t>0 ? rbf(t+1.0f) : rbf(expf(t)); return rbf(r+1e-6f); }
__global__ void kvz(const u16* qkv, float* kv, float* z){
  int h=blockIdx.x; int d=threadIdx.x/32, e=threadIdx.x%32;
  float acc=0, zz=0;
  for(int s=0;s<S;s++){ float pk=phi(bf2f(qkv[(long)s*3*D+D+h*DH+d])); float v=bf2f(qkv[(long)s*3*D+2*D+h*DH+e]); acc+=pk*v; zz+=pk; }
  kv[(h*32+d)*32+e]=rbf(acc); if(e==0) z[h*32+d]=rbf(zz);
}
__global__ void attn_o(const u16* qkv, const float* kv, const float* z, u16* o){
  int s=blockIdx.x; int h=threadIdx.x/32, e=threadIdx.x%32;
  float num=0, den=0;
  for(int d=0;d<32;d++){ float pq=phi(bf2f(qkv[(long)s*3*D+h*DH+d])); num+=pq*kv[(h*32+d)*32+e]; den+=pq*z[h*32+d]; }
  num=rbf(num); den=rbf(rbf(den)+1e-6f);
  o[(long)s*D+h*DH+e]=f2bf(num/den);
}
__global__ void kgen(const u16* lar, const u16* aim, const u16* cr, const u16* ci, const u16* ldt, float* Kk){
  int d=blockIdx.x;
  __shared__ float ar[NM], ai[NM], c_r[NM], c_i[NM];
  float dt=rbf(expf(bf2f(ldt[d])));
  for(int n=threadIdx.x;n<NM;n+=blockDim.x){
    ar[n]=-rbf(expf(bf2f(lar[d*NM+n])))*dt; ai[n]=bf2f(aim[d*NM+n])*dt;
    c_r[n]=bf2f(cr[d*NM+n])*dt; c_i[n]=bf2f(ci[d*NM+n])*dt;
  }
  __syncthreads();
  for(int l=threadIdx.x;l<S;l+=blockDim.x){
    float pos = ROUND_POS ? rbf((float)l) : (float)l;
    float acc=0;
    for(int n=0;n<NM;n++){ float m=expf(ar[n]*pos); float s,c; sincosf(ai[n]*pos,&s,&c); acc+= c_r[n]*m*c - c_i[n]*m*s; }
    Kk[(long)d*S+l]=2.0f*acc;
  }
}
__global__ void conv(const u16* x, const float* Kk, float* y){
  int d=blockIdx.x; __shared__ float u[S], k[S];
  for(int t=threadIdx.x;t<S;t+=blockDim.x){ u[t]=bf2f(x[(long)t*D+d]); k[t]=Kk[(long)d*S+t]; }
  __syncthreads();
  for(int t=threadIdx.x;t<S;t+=blockDim.x){ float acc=0; for(int l=0;l<=t;l++) acc+=k[l]*u[t-l]; y[(long)t*D+d]=rbf(acc); }
}
__global__ void mix(const u16* x, const u16* gw, const u16* dsk, const u16* a, const float* y, u16* out){
  int s=blockIdx.x; __shared__ float red[256];
  float p=0; for(int d=threadIdx.x;d<D;d+=256) p+=rbf(bf2f(x[(long)s*D+d])*bf2f(gw[d]));
  red[threadIdx.x]=p; __syncthreads();
  for(int o=128;o>0;o>>=1){ if(threadIdx.x<o) red[threadIdx.x]+=red[threadIdx.x+o]; __syncthreads(); }
  float sum=rbf(red[0]); float g=rbf(1.0f/(1.0f+expf(-sum))); float omg=rbf(1.0f-g);
  for(int d=threadIdx.x;d<D;d+=256){
    float b=rbf(y[(long)s*D+d]+rbf(bf2f(x[(long)s*D+d])*bf2f(dsk[d])));
    out[(long)s*D+d]=f2bf(rbf(g*bf2f(a[(long)s*D+d]))+rbf(omg*b));
  }
}
extern "C" void kernel_launch(void* const* d_in, const int* in_sizes, int n_in, void* d_out, int out_size, void* d_ws, size_t ws_size, hipStream_t stream){
  const u16* x=(const u16*)d_in[0]; const u16* wqkv=(const u16*)d_in[1]; const u16* wo=(const u16*)d_in[2];
  const u16* lar=(const u16*)d_in[4]; const u16* aim=(const u16*)d_in[5]; const u16* cr=(const u16*)d_in[6]; const u16* ci=(const u16*)d_in[7];
  const u16* ldt=(const u16*)d_in[8]; const u16* dsk=(const u16*)d_in[9]; const u16* gw=(const u16*)d_in[10];
  char* ws=(char*)d_ws;
  u16* qkv=(u16*)ws; ws+= (size_t)S*3*D*2;
  u16* o=(u16*)ws; ws+=(size_t)S*D*2;
  u16* a=(u16*)ws; ws+=(size_t)S*D*2;
  float* kv=(float*)ws; ws+=32*32*32*4;
  float* z=(float*)ws; ws+=32*32*4;
  float* Kk=(float*)ws; ws+=(size_t)D*S*4;
  float* y=(float*)ws; ws+=(size_t)D*S*4;
  gemm_naive<<<dim3(3*D/16,S/16),dim3(16,16),0,stream>>>(x,wqkv,qkv,S,3*D,D);
  kvz<<<H,1024,0,stream>>>(qkv,kv,z);
  attn_o<<<S,1024,0,stream>>>(qkv,kv,z,o);
  gemm_naive<<<dim3(D/16,S/16),dim3(16,16),0,stream>>>(o,wo,a,S,D,D);
  kgen<<<D,256,0,stream>>>(lar,aim,cr,ci,ldt,Kk);
  conv<<<D,256,0,stream>>>(x,Kk,y);
  mix<<<S,256,0,stream>>>(x,gw,dsk,a,y,(u16*)d_out);
}
```

```cpp
#include <hip/hip_runtime.h>
#include <stdint.h>
typedef unsigned short u16;
typedef unsigned long long u64;
typedef short s16x4 __attribute__((ext_vector_type(4)));
typedef __bf16 bf16x2 __attribute__((ext_vector_type(2)));
typedef __bf16 bf16x8 __attribute__((ext_vector_type(8)));
typedef _Float16 f16x2 __attribute__((ext_vector_type(2)));
typedef _Float16 f16x8 __attribute__((ext_vector_type(8)));
typedef float f32x2 __attribute__((ext_vector_type(2)));
typedef float f32x4 __attribute__((ext_vector_type(4)));
typedef unsigned u32x2 __attribute__((ext_vector_type(2)));
typedef unsigned u32x4 __attribute__((ext_vector_type(4)));
#define LAS __attribute__((address_space(3)))
#define DI __device__ __forceinline__

DI float bflo(unsigned v){ return __uint_as_float(v << 16); }
DI float bfhi(unsigned v){ return __uint_as_float(v & 0xffff0000u); }
DI unsigned pk_bf16(float a, float b){ f32x2 v = {a, b}; return __builtin_bit_cast(unsigned, __builtin_convertvector(v, bf16x2)); }
DI unsigned pk_f16(float a, float b){ f32x2 v = {a, b}; return __builtin_bit_cast(unsigned, __builtin_convertvector(v, f16x2)); }
DI float rbf(float f){ return bfhi(pk_bf16(f, f)); }
DI f32x4 mfma16(u32x4 a, u32x4 b, f32x4 c){ return __builtin_amdgcn_mfma_f32_16x16x32_bf16(__builtin_bit_cast(bf16x8, a), __builtin_bit_cast(bf16x8, b), c, 0, 0, 0); }
DI f32x4 mfma16h(u32x4 a, u32x4 b, f32x4 c){ return __builtin_amdgcn_mfma_f32_16x16x32_f16(__builtin_bit_cast(f16x8, a), __builtin_bit_cast(f16x8, b), c, 0, 0, 0); }
DI u32x4 lds_r128(unsigned addr){ return *(const LAS u32x4*)(uintptr_t)addr; }
DI float lds_rf(unsigned addr){ return *(const LAS float*)(uintptr_t)addr; }
DI u32x2 lds_tr64(unsigned addr){ return __builtin_bit_cast(u32x2, __builtin_amdgcn_ds_read_tr16_b64_v4i16((LAS s16x4*)(uintptr_t)addr)); }
DI u32x4 lds_tr128(unsigned a0, unsigned a1){ u32x2 lo = lds_tr64(a0), hi = lds_tr64(a1); return (u32x4){lo.x, lo.y, hi.x, hi.y}; }
DI void lds_w128(unsigned addr, u32x4 v){ *(LAS u32x4*)(uintptr_t)addr = v; }
DI void lds_w64(unsigned addr, u32x2 v){ *(LAS u32x2*)(uintptr_t)addr = v; }
DI void lds_wf(unsigned addr, float v){ *(LAS float*)(uintptr_t)addr = v; }
DI u32x4 mk_rsrc(const void* p, unsigned bytes){ uint64_t a = (uint64_t)p; u32x4 r; r.x = (unsigned)a; r.y = (unsigned)(a >> 32) & 0xffffu; r.z = bytes; r.w = 0x00020000u; return r; }
DI void glds16(u32x4 rsrc, unsigned voff, unsigned soff, unsigned ldsaddr){
  asm volatile("s_mov_b32 m0, %0\n\ts_nop 0\n\tbuffer_load_dwordx4 %1, %2, %3 offen lds" :: "s"(ldsaddr), "v"(voff), "s"(rsrc), "s"(soff) : "memory");
}
DI void glds16_dev(u32x4 rsrc, unsigned voff, unsigned soff, unsigned ldsaddr){
  asm volatile("s_mov_b32 m0, %0\n\ts_nop 0\n\tbuffer_load_dwordx4 %1, %2, %3 offen sc1 lds" :: "s"(ldsaddr), "v"(voff), "s"(rsrc), "s"(soff) : "memory");
}
template <int N> DI void wait_vm(){ asm volatile("s_waitcnt vmcnt(%0)" :: "n"(N) : "memory"); }
#define BAR() asm volatile("s_barrier" ::: "memory")
#define LGKM0() asm volatile("s_waitcnt lgkmcnt(0)" ::: "memory")
#define LGKM0_BAR() asm volatile("s_waitcnt lgkmcnt(0)\n\ts_barrier" ::: "memory")
#define SCHED() __builtin_amdgcn_sched_barrier(0)
#define SC1 16
typedef __amdgpu_buffer_rsrc_t rsrc_t;
DI u32x4 ld128_dev(rsrc_t r, unsigned voff, unsigned soff){ return __builtin_amdgcn_raw_buffer_load_b128(r, voff, soff, SC1); }
DI u32x2 ld64_dev(rsrc_t r, unsigned voff, unsigned soff){ return __builtin_amdgcn_raw_buffer_load_b64(r, voff, soff, SC1); }
DI unsigned ld32_dev(rsrc_t r, unsigned voff, unsigned soff){ return __builtin_amdgcn_raw_buffer_load_b32(r, voff, soff, SC1); }
DI void st128_dev(rsrc_t r, unsigned voff, unsigned soff, u32x4 v){ __builtin_amdgcn_raw_buffer_store_b128(v, r, voff, soff, SC1); }
DI void st64_dev(rsrc_t r, unsigned voff, unsigned soff, u32x2 v){ __builtin_amdgcn_raw_buffer_store_b64(v, r, voff, soff, SC1); }

constexpr float LOG2E = 1.4426950408889634f;
DI float phi(float t){
  float e = __builtin_amdgcn_fmed3f(__builtin_amdgcn_exp2f(t * LOG2E), 0.0f, 1.0f);
  float m; asm("v_max_f32 %0, 0, %1" : "=v"(m) : "v"(t));
  return (m + e) + 1e-6f;
}
DI float xsum16_32(float x){
  unsigned u = __float_as_uint(x);
  auto a = __builtin_amdgcn_permlane16_swap(u, u, false, false);
  float s = __uint_as_float(a[0]) + __uint_as_float(a[1]);
  u = __float_as_uint(s);
  auto b = __builtin_amdgcn_permlane32_swap(u, u, false, false);
  return __uint_as_float(b[0]) + __uint_as_float(b[1]);
}

constexpr unsigned WS_E = 0, WS_F1 = 4096, WS_F3 = WS_F1 + 131072, WS_F4 = WS_F3 + 131072, WS_GP = WS_F4 + 131072,
                   WS_P2 = WS_GP + 131072, WS_P1 = WS_P2 + 131072, P1_REC = 8448, WS_XT = WS_P1 + 1024 * P1_REC, XT_ROW = 4160,
                   WS_O = WS_XT + 1024 * XT_ROW + 4096, WS_BT = WS_O + 4194304, WS_END = WS_BT + 4194304;
constexpr unsigned ST_BYTES = 40960, A_BYTES = 16384, L_R1 = 40960, L_X = 122880, X_STR = 10240, UC_STR = 16640, LDS_BYTES = 163840;
constexpr unsigned SPIN_CAP = 1u << 17;

struct Args { const u16 *x, *w, *wo, *lar, *aim, *cr, *ci, *ldt, *dsk, *gw; u16* out; char* ws; };

#define STAMP(i)

struct cplx2 { f32x2 re, im; };
DI cplx2 cmul(cplx2 a, cplx2 b){ cplx2 r; r.re = a.re * b.re - a.im * b.im; r.im = a.re * b.im + a.im * b.re; return r; }
DI cplx2 cpow(f32x2 a2, f32x2 ph, float p){
  cplx2 r;
  #pragma unroll
  for (int m = 0; m < 2; ++m) {
    float mag = __builtin_amdgcn_exp2f(a2[m] * p), rev = __builtin_amdgcn_fractf(ph[m] * p);
    r.re[m] = mag * __builtin_amdgcn_cosf(rev); r.im[m] = mag * __builtin_amdgcn_sinf(rev);
  }
  return r;
}
constexpr unsigned wo_unit(int h){ return h < 8 ? 4096u * h : ((h - 8) % 6 < 4 ? L_R1 + UC_STR * ((h - 8) / 6) + 4096u * ((h - 8) % 6) : L_X + X_STR * ((h - 8) / 6) + 4096u * ((h - 8) % 6 - 4)); }

DI void poll_flags(rsrc_t rw, unsigned voff, unsigned tlo, unsigned thi){
  _Pragma("unroll 1") for (unsigned sp = 0; sp < SPIN_CAP; ++sp) {
    u32x2 v = ld64_dev(rw, voff, 0);
    if (__all(v.x == tlo && v.y == thi)) break;
    __builtin_amdgcn_s_sleep(1);
    asm volatile("" ::: "memory");
  }
  asm volatile("" ::: "memory");
}

__global__ __launch_bounds__(512, 2) void fused_kernel(Args a){
  extern __shared__ __attribute__((aligned(16))) char smem[];
  const int tid = threadIdx.x, lane = tid & 63;
  const int wave = __builtin_amdgcn_readfirstlane(tid >> 6);
  const int b = blockIdx.x, xcd = b & 7, idx = b >> 3;
  const int mt = (xcd & 1) * 8 + (idx & 7), hp = (xcd >> 1) * 4 + (idx >> 3);
  const int mt2 = 2 * xcd + (idx >> 4), nt = idx & 15;
  const unsigned lds0 = (unsigned)(uintptr_t)(LAS char*)smem;
  const u64 tag = *(const u64*)(a.ws + WS_E) + 1ull;
  const unsigned tlo = (unsigned)tag, thi = (unsigned)(tag >> 32);
  const rsrc_t rw = __builtin_amdgcn_make_buffer_rsrc(a.ws, 0, WS_END, 0x00020000);
  const u32x4 rws = mk_rsrc(a.ws, WS_END);
  const int g = lane >> 4, l16 = lane & 15, q4 = l16 >> 2, p4 = lane & 3;
  const unsigned r8 = lane >> 3, c8 = lane & 7;
  STAMP(0);

  if (wave >= 4) {
    const int lw = wave - 4, d = b * 4 + lw;
    const u32x4 rx = mk_rsrc(a.x, 2048u * 1024u * 2u), rwq = mk_rsrc(a.w, 1024u * 3072u * 2u), rwo = mk_rsrc(a.wo, 1024u * 1024u * 2u);
    unsigned p_lar, p_aim, p_cr, p_ci, p_ldt, p_dsk; u32x4 gw0, gw1;
    {
      const unsigned po = (unsigned)(d * 128 + 2 * lane) * 2u, dd = (unsigned)d * 2u, go = (unsigned)(hp * 64 + 8 * g) * 2u;
      asm volatile("global_load_dword %0, %8, %11\n\tglobal_load_dword %1, %8, %12\n\tglobal_load_dword %2, %8, %13\n\tglobal_load_dword %3, %8, %14\n\t"
                   "global_load_ushort %4, %9, %15\n\tglobal_load_ushort %5, %9, %16\n\tglobal_load_dwordx4 %6, %10, %17\n\tglobal_load_dwordx4 %7, %10, %17 offset:64"
                   : "=&v"(p_lar), "=&v"(p_aim), "=&v"(p_cr), "=&v"(p_ci), "=&v"(p_ldt), "=&v"(p_dsk), "=&v"(gw0), "=&v"(gw1)
                   : "v"(po), "v"(dd), "v"(go), "s"(a.lar), "s"(a.aim), "s"(a.cr), "s"(a.ci), "s"(a.ldt), "s"(a.dsk), "s"(a.gw) : "memory");
    }
    const unsigned va0 = r8 * 2048u + ((c8 ^ (r8 >> 1)) << 4), va1 = r8 * 2048u + ((c8 ^ (4u + (r8 >> 1))) << 4);
    const unsigned sw0 = (c8 ^ (((r8 >> 1) & 1u) << 1)) << 4, sw1 = sw0 ^ 64u;
    const unsigned vb0 = r8 * 6144u + sw0, vb1 = r8 * 6144u + sw1;
    const unsigned sa = (unsigned)(mt * 128 + lw * 32) * 2048u, sb = (unsigned)(lw * 16) * 6144u + (unsigned)hp * 128u;
    const unsigned xreg = lds0 + L_X + lw * X_STR, ureg = lds0 + L_R1 + lw * UC_STR;
    auto issue = [&](int s, unsigned buf){
      const unsigned la = lds0 + buf + lw * 4096u, lb = lds0 + buf + A_BYTES + lw * 2048u;
      const unsigned oa = sa + s * 128u, ob = sb + s * (64u * 6144u);
      glds16(rx, va0, oa, la);
      glds16(rwq, vb0, ob, lb);
      glds16(rwq, vb0, ob + 2048u, lb + 8192u);
      glds16(rwq, vb0, ob + 4096u, lb + 16384u);
      glds16(rx, va1, oa + 16384u, la + 1024u);
      glds16(rwq, vb1, ob + 8u * 6144u, lb + 1024u);
      glds16(rwq, vb1, ob + 8u * 6144u + 2048u, lb + 8192u + 1024u);
      glds16(rwq, vb1, ob + 8u * 6144u + 4096u, lb + 16384u + 1024u);
      glds16(rx, va0, oa + 32768u, la + 2048u);
      glds16(rx, va1, oa + 49152u, la + 3072u);
    };
    issue(0, 0);
    issue(1, ST_BYTES);
    asm volatile("s_waitcnt vmcnt(10) lgkmcnt(0)" : "+v"(p_lar), "+v"(p_aim), "+v"(p_cr), "+v"(p_ci), "+v"(p_ldt), "+v"(p_dsk), "+v"(gw0), "+v"(gw1) : "s"(tlo), "s"(thi) : "memory");
    BAR();
    {
      const unsigned ox = sa + hp * 128u, vx0 = r8 * 2048u + sw0, vx1 = r8 * 2048u + sw1;
      glds16(rx, vx0, ox, xreg); glds16(rx, vx1, ox + 16384u, xreg + 1024u); glds16(rx, vx0, ox + 32768u, xreg + 2048u); glds16(rx, vx1, ox + 49152u, xreg + 3072u);
    }
    issue(2, 2 * ST_BYTES);
    wait_vm<10>(); BAR();
    {
      const unsigned ta = xreg + (unsigned)(8 * g + q4) * 128u + ((unsigned)(((q4 >> 1) & 1) | ((g & 1) << 1)) << 5) + 8u * p4;
      const unsigned vo = WS_XT + (unsigned)(hp * 64 + l16) * XT_ROW + (unsigned)(16 + mt * 128 + 32 * lw + 8 * g) * 2u;
      #pragma unroll
      for (int cb = 0; cb < 4; ++cb) st128_dev(rw, vo, cb * 16u * XT_ROW, lds_tr128(ta ^ (cb << 5), (ta ^ (cb << 5)) + 512u));
      const unsigned fr = (unsigned)(((l16 >> 1) & 1) | (((l16 >> 3) & 1) << 1)) << 1;
      const unsigned ga = xreg + l16 * 128u + ((g ^ fr) << 4);
      f32x4 g0 = {0, 0, 0, 0}, g1 = {0, 0, 0, 0};
      g0 = mfma16(lds_r128(ga), gw0, g0); g0 = mfma16(lds_r128(ga ^ 64u), gw1, g0);
      g1 = mfma16(lds_r128(ga + 2048u), gw0, g1); g1 = mfma16(lds_r128((ga ^ 64u) + 2048u), gw1, g1);
      if (l16 < 2) st128_dev(rw, WS_GP + (unsigned)((mt * 16 + hp) * 128 + 32 * lw + 16 * l16 + 4 * g) * 4u, 0, __builtin_bit_cast(u32x4, l16 ? g1 : g0));
      if (mt == 0 && lw < 2) st128_dev(rw, WS_XT + (unsigned)(hp * 64 + lane) * XT_ROW + lw * 16u, 0, (u32x4){0, 0, 0, 0});
    }
    issue(3, 0);
    wait_vm<10>(); BAR();
    if (lane == 0) st64_dev(rw, WS_F1 + (unsigned)((hp * 16 + mt) * 4 + lw) * 128u, 0, (u32x2){tlo, thi});
    unsigned buf = ST_BYTES;
    for (int s = 2; s < 13; ++s) {
      issue(s + 2, buf);
      buf = (buf == 2 * ST_BYTES) ? 0 : buf + ST_BYTES;
      wait_vm<10>(); BAR();
    }
    issue(15, 0);
    STAMP(1);
    const float dt = rbf(__builtin_amdgcn_exp2f(bflo(p_ldt) * LOG2E));
    f32x2 a2, ph; cplx2 C;
    a2[0] = -rbf(__builtin_amdgcn_exp2f(bflo(p_lar) * LOG2E)) * dt * LOG2E; a2[1] = -rbf(__builtin_amdgcn_exp2f(bfhi(p_lar) * LOG2E)) * dt * LOG2E;
    ph[0] = bflo(p_aim) * dt * 0.15915494309189535f; ph[1] = bfhi(p_aim) * dt * 0.15915494309189535f;
    C.re = (f32x2){bflo(p_cr), bfhi(p_cr)}; C.im = (f32x2){bflo(p_ci), bfhi(p_ci)};
    float unscale;
    {
      float mx = fmaxf(fmaxf(fabsf(C.re[0]), fabsf(C.re[1])), fmaxf(fabsf(C.im[0]), fabsf(C.im[1])));
      #pragma unroll
      for (int o = 1; o < 64; o <<= 1) mx = fmaxf(mx, __shfl_xor(mx, o));
      int e = __builtin_amdgcn_frexp_expf(mx); e = e < -100 ? -100 : (e > 100 ? 100 : e);
      const float sc = __builtin_ldexpf(1.0f, 14 - e);
      C.re *= sc; C.im *= sc;
      unscale = 2.0f * dt * __builtin_ldexpf(1.0f, e - 28);
    }
    const cplx2 w1 = cpow(a2, ph, 1.0f), w256 = cpow(a2, ph, 256.0f);
    cplx2 w16 = cpow(a2, ph, 16.0f); w16.im = -w16.im;
    const unsigned kw = xreg + lane * 8u, kr = xreg + l16 * 544u + g * 16u;
    auto put = [&](int j, cplx2 v){ lds_w64(kw + j * 544u, (u32x2){pk_f16(v.re[0], v.im[0]), pk_f16(v.re[1], v.im[1])}); };
    SCHED(); wait_vm<10>(); BAR(); SCHED();
    u32x4 qf[8];
    {
      cplx2 v; v.re = (f32x2){16384.f, 16384.f}; v.im = (f32x2){0.f, 0.f};
      #pragma unroll
      for (int j = 0; j < 16; ++j) { put(j, v); if (j < 15) v = cmul(v, w16); }
      #pragma unroll
      for (int ks = 0; ks < 8; ++ks) qf[ks] = lds_r128(kr + ks * 64u);
    }
    SCHED(); asm volatile("s_waitcnt vmcnt(0) lgkmcnt(0)\n\ts_barrier" ::: "memory"); SCHED();
    poll_flags(rw, WS_F1 + (unsigned)((d >> 6) * 64 + lane) * 128u, tlo, thi);
    {
      const unsigned so = WS_XT + (unsigned)d * XT_ROW;
      #pragma unroll
      for (int sg = 0; sg < 4; ++sg) {
        #pragma unroll
        for (int i = 0; i < 4; ++i) glds16_dev(rws, lane * 16u, so + 2u * sg + 1024u * i, ureg + sg * XT_ROW + 1024u * i);
      }
      if (lane < 2) {
        #pragma unroll
        for (int sg = 0; sg < 4; ++sg) glds16_dev(rws, lane * 16u, so + 2u * sg + 4096u, ureg + sg * XT_ROW + 4096u);
      }
    }
    f32x4 k1 = {0, 0, 0, 0}, k2 = {0, 0, 0, 0}, k3 = {0, 0, 0, 0};
    {
      cplx2 v = C;
      #pragma unroll
      for (int j = 0; j < 16; ++j) { put(j, v); if (j < 15) v = cmul(v, w1); }
      #pragma unroll
      for (int ks = 0; ks < 8; ++ks) k1 = mfma16h(lds_r128(kr + ks * 64u), qf[ks], k1);
    }
    SCHED(); BAR(); SCHED();
    const unsigned vwo = r8 * 2048u + ((c8 ^ (((r8 >> 1) & 3u) << 1)) << 4);
    #pragma unroll
    for (int i = 0; i < 8; ++i) glds16(rwo, vwo, (unsigned)(lw * 64 + i * 8) * 2048u + nt * 128u, lds0 + lw * 8192u + i * 1024u);
    {
      const cplx2 w2 = cmul(w1, w1), w4 = cmul(w2, w2), w8 = cmul(w4, w4);
      cplx2 c256 = cmul(C, w256), v = c256;
      #pragma unroll
      for (int j = 0; j < 8; ++j) { put(j, v); if (j < 7) v = cmul(v, w2); }
      #pragma unroll
      for (int hh = 0; hh < 2; ++hh) {
        c256 = cmul(c256, w256); v = c256;
        #pragma unroll
        for (int j = 0; j < 4; ++j) { put(8 + 4 * hh + j, v); if (j < 3) v = cmul(v, w4); }
      }
      #pragma unroll
      for (int ks = 0; ks < 8; ++ks) k2 = mfma16h(lds_r128(kr + ks * 64u), qf[ks], k2);
      #pragma unroll
      for (int bb = 0; bb < 4; ++bb) { c256 = cmul(c256, w256); put(2 * bb, c256); put(2 * bb + 1, cmul(c256, w8)); }
      put(8, cmul(c256, w256));
      #pragma unroll
      for (int ks = 0; ks < 8; ++ks) k3 = mfma16h(lds_r128(kr + ks * 64u), qf[ks], k3);
    }
    {
      k1 *= unscale; k2 *= unscale; k3 *= unscale;
      if (lane == 0) k1[0] += bflo(p_dsk);
      const unsigned T = xreg + 4608u;
      lds_w64(xreg + (unsigned)(2044 - 16 * l16 - 4 * g) * 2u, (u32x2){pk_bf16(k1[3], k1[2]), pk_bf16(k1[1], k1[0])});
      if (lane < 30) lds_w128(xreg + 4096u + lane * 16u, (u32x4){0, 0, 0, 0});
      lds_w128(T + (unsigned)(g < 2 ? 8 * l16 + 4 * g : 128 + 64 * (g - 2) + 4 * l16) * 4u, __builtin_bit_cast(u32x4, k2));
      if (g < 2) {
        lds_w64(T + (unsigned)(256 + 64 * g + 2 * l16) * 4u, (u32x2){__float_as_uint(k3[0]), __float_as_uint(k3[1])});
        lds_w64(T + (unsigned)(256 + 64 * g + 32 + 2 * l16) * 4u, (u32x2){__float_as_uint(k3[2]), __float_as_uint(k3[3])});
      }
      if (lane == 32) lds_wf(T + 384u * 4u, k3[0]);
      LGKM0();
      {
        const unsigned c = lane & 31;
        const f32x4 v = __builtin_bit_cast(f32x4, lds_r128(T + c * 16u)); const float v4 = lds_rf(T + c * 16u + 16u);
        const float c0 = lds_rf(T + (128u + 2u * lane) * 4u), c1 = lds_rf(T + (129u + 2u * lane) * 4u), c2 = lds_rf(T + (130u + 2u * lane) * 4u);
        const float d0 = lds_rf(T + (256u + 2u * lane) * 4u), d1 = lds_rf(T + (257u + 2u * lane) * 4u), d2 = lds_rf(T + (258u + 2u * lane) * 4u);
        lds_w128(xreg + (1784u - 8u * c) * 2u, (u32x4){pk_bf16(v4, v[3]), pk_bf16(v[2], v[2]), pk_bf16(v[2], v[1]), pk_bf16(v[0], v[0])});
        lds_w128(xreg + (1528u - 8u * lane) * 2u, (u32x4){pk_bf16(c2, c2), pk_bf16(c1, c1), pk_bf16(c1, c0), pk_bf16(c0, c0)});
        const unsigned aa = pk_bf16(d0, d0), bb = pk_bf16(d1, d1), ee = pk_bf16(d2, d2);
        lds_w128(xreg + (1016u - 16u * lane) * 2u, (u32x4){bb, pk_bf16(d1, d0), aa, aa});
        lds_w128(xreg + (1008u - 16u * lane) * 2u, (u32x4){ee, ee, bb, bb});
      }
    }
    asm volatile("s_waitcnt vmcnt(8) lgkmcnt(0)" ::: "memory");
    STAMP(2);
    {
      f32x4 acc[8];
      #pragma unroll
      for (int c = 0; c < 8; ++c) acc[c] = (f32x4){0, 0, 0, 0};
      const unsigned aA = ureg + (unsigned)((1 + q4) & 3) * XT_ROW + (unsigned)(8 * g + 4 * p4 + (q4 == 3 ? 4 : 0)) * 2u;
      const unsigned aB = xreg + (unsigned)(2032 - 256 * 7 - 16 * l16 + 8 * g) * 2u;
      u32x4 fa[2][8], fb[2][8];
      auto ldf = [&](int set, int r){
        #pragma unroll
        for (int q = 0; q < 8; ++q) {
          fb[set][q] = lds_r128(aB + r * 64u + (7 - q) * 512u);
          fa[set][q] = lds_tr128(aA + r * 64u + q * 512u, aA + r * 64u + q * 512u + 8u);
        }
      };
      auto mm = [&](int set){
        #pragma unroll
        for (int q = 0; q < 8; ++q)
          #pragma unroll
          for (int c = q; c < 8; ++c) acc[c] = mfma16(fa[set][q], fb[set][c - q], acc[c]);
      };
      auto ilv = [&](){
        #pragma unroll
        for (int i = 0; i < 24; ++i) { __builtin_amdgcn_sched_group_barrier(0x008, 1, 0); __builtin_amdgcn_sched_group_barrier(0x100, 1, 0); }
        __builtin_amdgcn_sched_group_barrier(0x008, 12, 0);
      };
      ldf(0, 0);
      for (int r = 0; r < 8; r += 2) {
        SCHED();
        ldf(1, r + 1); mm(0); ilv(); SCHED();
        ldf(0, (r + 2) & 7); mm(1); ilv();
      }
      SCHED();
      LGKM0();
      const unsigned vo = WS_BT + (unsigned)d * 4096u + (unsigned)(16 * l16 + 4 * g) * 2u;
      #pragma unroll
      for (int c = 0; c < 8; ++c) st64_dev(rw, vo, c * 512u, (u32x2){pk_bf16(acc[c][0], acc[c][1]), pk_bf16(acc[c][2], acc[c][3])});
    }
    STAMP(3);
    #pragma unroll
    for (int i = 0; i < 24; ++i) glds16(rwo, vwo, (unsigned)((8 + 6 * lw) * 32 + i * 8) * 2048u + nt * 128u, (i < 16 ? ureg : xreg - 16384u) + i * 1024u);
    wait_vm<24>();
    if (lane == 0) st64_dev(rw, WS_F4 + (unsigned)d * 128u, 0, (u32x2){tlo, thi});
    wait_vm<0>();
  } else {
    const int wr = wave >> 1, wc = wave & 1;
    const unsigned aA = lds0 + (unsigned)(wr * 64 + l16) * 128u + ((unsigned)(g ^ ((l16 >> 1) & 7)) << 4);
    const unsigned aB0 = lds0 + A_BYTES + (unsigned)(8 * g + q4) * 128u + ((unsigned)((2 * wc) ^ (((q4 >> 1) & 1) | ((g & 1) << 1))) << 5) + 8u * p4;
    const unsigned aB1 = aB0 ^ 32u;
    f32x4 acc[3][4][2];
    #pragma unroll
    for (int i = 0; i < 3; ++i)
      #pragma unroll
      for (int j = 0; j < 4; ++j) { acc[i][j][0] = (f32x4){0, 0, 0, 0}; acc[i][j][1] = (f32x4){0, 0, 0, 0}; asm volatile("" : "+v"(acc[i][j][0]), "+v"(acc[i][j][1])); }
    u32x4 fa[2][4], fb[2][6];
    auto ldfr = [&](int set, unsigned buf, int ks){
      const unsigned pa = (aA + buf) ^ (unsigned)(ks << 6);
      #pragma unroll
      for (int rt = 0; rt < 4; ++rt) fa[set][rt] = lds_r128(pa + rt * 2048u);
      #pragma unroll
      for (int sg = 0; sg < 3; ++sg) {
        const unsigned o = buf + sg * 8192u + ks * 4096u;
        fb[set][2 * sg] = lds_tr128(aB0 + o, aB0 + o + 512u);
        fb[set][2 * sg + 1] = lds_tr128(aB1 + o, aB1 + o + 512u);
      }
    };
    auto mm = [&](int set){
      #pragma unroll
      for (int sg = 0; sg < 3; ++sg)
        #pragma unroll
        for (int ct = 0; ct < 2; ++ct)
          #pragma unroll
          for (int rt = 0; rt < 4; ++rt)
            acc[sg][rt][ct] = sg == 0 ? mfma16(fb[set][ct], fa[set][rt], acc[sg][rt][ct]) : mfma16(fa[set][rt], fb[set][2 * sg + ct], acc[sg][rt][ct]);
    };
    auto ilv = [&](){
      #pragma unroll
      for (int i = 0; i < 16; ++i) { __builtin_amdgcn_sched_group_barrier(0x008, 1, 0); __builtin_amdgcn_sched_group_barrier(0x100, 1, 0); }
      __builtin_amdgcn_sched_group_barrier(0x008, 8, 0);
    };
    asm volatile("s_waitcnt lgkmcnt(0)\n\ts_barrier" :: "s"(tlo), "s"(thi) : "memory");
    STAMP(1);
    ldfr(0, 0, 0);
    unsigned buf = 0;
    for (int s = 0; s < 16; ++s) {
      SCHED();
      ldfr(1, buf, 1); mm(0); ilv(); SCHED();
      buf = (buf == 2 * ST_BYTES) ? 0 : buf + ST_BYTES;
      LGKM0_BAR(); SCHED();
      ldfr(0, buf, 0); mm(1); ilv();
    }
    SCHED();
    STAMP(2);
    const int head = hp * 2 + wc, prod = mt * 2 + wr;
    {
      f32x4 T[2][2], Z[2];
      #pragma unroll
      for (int i = 0; i < 2; ++i) { T[i][0] = (f32x4){0, 0, 0, 0}; T[i][1] = (f32x4){0, 0, 0, 0}; Z[i] = (f32x4){0, 0, 0, 0}; }
      const u32x4 ones = {0x3f803f80u, 0x3f803f80u, 0x3f803f80u, 0x3f803f80u};
      #pragma unroll
      for (int rp = 0; rp < 2; ++rp) {
        u32x4 pk[2], vv[2];
        #pragma unroll
        for (int t = 0; t < 2; ++t) {
          const f32x4 k0 = acc[1][2 * rp][t], k1 = acc[1][2 * rp + 1][t], v0 = acc[2][2 * rp][t], v1 = acc[2][2 * rp + 1][t];
          pk[t] = (u32x4){pk_bf16(phi(k0[0]), phi(k0[1])), pk_bf16(phi(k0[2]), phi(k0[3])), pk_bf16(phi(k1[0]), phi(k1[1])), pk_bf16(phi(k1[2]), phi(k1[3]))};
          vv[t] = (u32x4){pk_bf16(v0[0], v0[1]), pk_bf16(v0[2], v0[3]), pk_bf16(v1[0], v1[1]), pk_bf16(v1[2], v1[3])};
        }
        #pragma unroll
        for (int dt = 0; dt < 2; ++dt) {
          T[dt][0] = mfma16(pk[dt], vv[0], T[dt][0]); T[dt][1] = mfma16(pk[dt], vv[1], T[dt][1]); Z[dt] = mfma16(pk[dt], ones, Z[dt]);
        }
      }
      const unsigned rec = WS_P1 + (unsigned)(head * 32 + prod) * P1_REC;
      #pragma unroll
      for (int vp = 0; vp < 8; ++vp) {
        const f32x4 t = T[(vp >> 1) & 1][vp >> 2];
        st128_dev(rw, lane * 16u, rec + vp * 1024u, (u32x4){__float_as_uint(t[2 * (vp & 1)]), tlo, __float_as_uint(t[2 * (vp & 1) + 1]), tlo});
      }
      const f32x4 zs = (l16 & 2) ? Z[1] : Z[0];
      const float z0 = (l16 & 1) ? zs[2] : zs[0], z1 = (l16 & 1) ? zs[3] : zs[1];
      if (l16 < 4) st128_dev(rw, (unsigned)(g * 4 + l16) * 16u, rec + 8192u, (u32x4){__float_as_uint(z0), tlo, __float_as_uint(z1), tlo});
    }
    SCHED();
    STAMP(3);
    u32x4 pq[4];
    #pragma unroll
    for (int st = 0; st < 4; ++st) {
      const f32x4 q0 = acc[0][st][0], q1 = acc[0][st][1];
      pq[st] = (u32x4){pk_bf16(phi(q0[0]), phi(q0[1])), pk_bf16(phi(q0[2]), phi(q0[3])), pk_bf16(phi(q1[0]), phi(q1[1])), pk_bf16(phi(q1[2]), phi(q1[3]))};
      asm volatile("" : "+v"(pq[st]));
    }
    SCHED();
    u32x4 zf;
    {
      const unsigned base = WS_P1 + (unsigned)(head * 32) * P1_REC;
      const unsigned vz = (unsigned)g * P1_REC + l16 * 16u, vs = vz + (unsigned)(prod >> 2) * 1024u + (unsigned)(prod & 3) * 256u;
      float s0, s1, z0, z1;
      _Pragma("unroll 1") for (unsigned sp = 0; sp < SPIN_CAP; ++sp) {
        u32x4 v[8], w[8];
        #pragma unroll
        for (int i = 0; i < 8; ++i) { v[i] = ld128_dev(rw, vs, base + i * 4u * P1_REC); w[i] = ld128_dev(rw, vz, base + i * 4u * P1_REC + 8192u); }
        unsigned bad = 0; s0 = s1 = z0 = z1 = 0.f;
        #pragma unroll
        for (int i = 0; i < 8; ++i) {
          bad |= (v[i].y ^ tlo) | (v[i].w ^ tlo) | (w[i].y ^ tlo) | (w[i].w ^ tlo);
          s0 += __uint_as_float(v[i].x); s1 += __uint_as_float(v[i].z); z0 += __uint_as_float(w[i].x); z1 += __uint_as_float(w[i].z);
        }
        if (__all(bad == 0)) break;
        __builtin_amdgcn_s_sleep(1);
        asm volatile("" ::: "memory");
      }
      s0 = xsum16_32(s0); s1 = xsum16_32(s1);
      if (lane < 16) st64_dev(rw, WS_P2 + (unsigned)head * 4096u + (unsigned)(prod * 16 + lane) * 8u, 0, (u32x2){pk_bf16(s0, s1), tlo});
      const unsigned zp = pk_bf16(xsum16_32(z0), xsum16_32(z1));
      #pragma unroll
      for (int j = 0; j < 4; ++j) zf[j] = (unsigned)__builtin_amdgcn_ds_bpermute((g * 4 + j) * 4, (int)zp);
    }
    STAMP(4);
    u32x4 kvf[2];
    _Pragma("unroll 1") for (unsigned sp = 0; sp < SPIN_CAP; ++sp) {
      unsigned bad = 0;
      #pragma unroll
      for (int vp = 0; vp < 8; ++vp) { const u32x2 v = ld64_dev(rw, WS_P2 + (unsigned)head * 4096u + lane * 8u + vp * 512u, 0); kvf[vp >> 2][vp & 3] = v.x; bad |= v.y ^ tlo; }
      if (__all(bad == 0)) break;
      __builtin_amdgcn_s_sleep(1);
      asm volatile("" ::: "memory");
    }
    STAMP(5);
    #pragma unroll
    for (int st = 0; st < 4; ++st) {
      const f32x4 zero = {0, 0, 0, 0};
      const f32x4 o0 = mfma16(kvf[0], pq[st], zero), o1 = mfma16(kvf[1], pq[st], zero), dn = mfma16(zf, pq[st], zero);
      const float inv = __builtin_amdgcn_rcpf(dn[0] + 1e-6f);
      st128_dev(rw, lane * 16u, WS_O + (unsigned)((mt * 8 + wr * 4 + st) * 32 + head) * 1024u,
                (u32x4){pk_bf16(o0[0] * inv, o0[1] * inv), pk_bf16(o0[2] * inv, o0[3] * inv), pk_bf16(o1[0] * inv, o1[1] * inv), pk_bf16(o1[2] * inv, o1[3] * inv)});
    }
    wait_vm<0>();
    if (lane == 0) st64_dev(rw, WS_F3 + (unsigned)((mt * 16 + hp) * 4 + wave) * 128u, 0, (u32x2){tlo, thi});
  }
  STAMP(6);
  BAR();
  STAMP(7);
  {
    const int rg = wave & 3, kh = wave >> 2;
    poll_flags(rw, WS_F3 + (unsigned)((mt2 * 16 + ((lane & 31) >> 1)) * 4 + (rg >> 1) * 2 + (lane & 1)) * 128u, tlo, thi);
    STAMP(8);
    if (b == 0 && tid == 0) *(u64*)(a.ws + WS_E) = tag;
    u32x4 of[16][2];
    const unsigned ob = WS_O + (unsigned)((mt2 * 8 + rg * 2) * 32 + kh * 16) * 1024u;
    #pragma unroll
    for (int i = 0; i < 16; ++i) { of[i][0] = ld128_dev(rw, lane * 16u, ob + i * 1024u); of[i][1] = ld128_dev(rw, lane * 16u, ob + 32768u + i * 1024u); }
    const int row = rg * 32 + kh * 16 + l16;
    float gs[4];
    #pragma unroll
    for (int i = 0; i < 4; ++i) gs[i] = __uint_as_float(ld32_dev(rw, WS_GP + (unsigned)((mt2 * 16 + 4 * g + i) * 128 + row) * 4u, 0));
    SCHED();
    f32x4 acc[2][4];
    #pragma unroll
    for (int i = 0; i < 4; ++i) { acc[0][i] = (f32x4){0, 0, 0, 0}; acc[1][i] = (f32x4){0, 0, 0, 0}; }
    const unsigned aW = lds0 + (unsigned)(4 * g + q4) * 128u + ((unsigned)((2 * g + (q4 >> 1)) & 3) << 5) + 8u * p4;
    u32x4 wf[2][4];
    auto ldw = [&](int set, int i){
      const unsigned u = aW + (kh ? wo_unit(16 + i) : wo_unit(i));
      #pragma unroll
      for (int ct = 0; ct < 4; ++ct) wf[set][ct] = lds_tr128(u ^ (ct << 5), (u ^ (ct << 5)) + 2048u);
    };
    ldw(0, 0);
    #pragma unroll
    for (int i = 0; i < 16; ++i) {
      if (i < 15) ldw((i + 1) & 1, i + 1);
      #pragma unroll
      for (int ct = 0; ct < 4; ++ct) { acc[0][ct] = mfma16(wf[i & 1][ct], of[i][0], acc[0][ct]); acc[1][ct] = mfma16(wf[i & 1][ct], of[i][1], acc[1][ct]); }
      SCHED();
    }
    STAMP(9);
    poll_flags(rw, WS_F4 + (unsigned)(nt * 64 + lane) * 128u, tlo, thi);
    const unsigned bst = lds0 + (kh ? L_R1 + 4u * UC_STR : 32768u) + rg * 2048u;
    {
      const unsigned vo = (unsigned)(lane >> 1) * 4096u + (lane & 1) * 16u, so = WS_BT + (unsigned)(nt * 64) * 4096u + (unsigned)(mt2 * 128 + rg * 32 + kh * 16) * 2u;
      glds16_dev(rws, vo, so, bst); glds16_dev(rws, vo, so + 32u * 4096u, bst + 1024u);
    }
    LGKM0_BAR();
    const unsigned ex = lds0 + lane * 16u;
    #pragma unroll
    for (int ct = 0; ct < 4; ++ct) lds_w128(ex + wave * 4096u + ct * 1024u, __builtin_bit_cast(u32x4, kh ? acc[0][ct] : acc[1][ct]));
    const float lg = rbf(xsum16_32((gs[0] + gs[1]) + (gs[2] + gs[3])));
    const float gt = rbf(__builtin_amdgcn_rcpf(1.0f + __builtin_amdgcn_exp2f(-lg * LOG2E))), og = rbf(1.0f - gt);
    asm volatile("s_waitcnt vmcnt(0) lgkmcnt(0)\n\ts_barrier" ::: "memory");
    u16* op = a.out + (size_t)(mt2 * 128 + row) * 1024 + nt * 64 + 4 * g;
    #pragma unroll
    for (int ct = 0; ct < 4; ++ct) {
      const f32x4 av = (kh ? acc[1][ct] : acc[0][ct]) + __builtin_bit_cast(f32x4, lds_r128(ex + (wave ^ 4) * 4096u + ct * 1024u));
      const u32x2 bv = lds_tr64(bst + (unsigned)(16 * ct + 4 * g + q4) * 32u + 8u * p4);
      *(u32x2*)(op + 16 * ct) = (u32x2){pk_bf16(gt * av[0] + og * bflo(bv.x), gt * av[1] + og * bfhi(bv.x)), pk_bf16(gt * av[2] + og * bflo(bv.y), gt * av[3] + og * bfhi(bv.y))};
    }
    STAMP(10);
  }
}

extern "C" void kernel_launch(void* const* d_in, const int* in_sizes, int n_in, void* d_out, int out_size, void* d_ws, size_t ws_size, hipStream_t stream){
  Args a;
  a.x = (const u16*)d_in[0]; a.w = (const u16*)d_in[1]; a.wo = (const u16*)d_in[2];
  a.lar = (const u16*)d_in[4]; a.aim = (const u16*)d_in[5]; a.cr = (const u16*)d_in[6]; a.ci = (const u16*)d_in[7];
  a.ldt = (const u16*)d_in[8]; a.dsk = (const u16*)d_in[9]; a.gw = (const u16*)d_in[10];
  a.out = (u16*)d_out; a.ws = (char*)d_ws;
  (void)hipFuncSetAttribute((const void*)fused_kernel, hipFuncAttributeMaxDynamicSharedMemorySize, LDS_BYTES);
  fused_kernel<<<256, 512, LDS_BYTES, stream>>>(a);
}
```

```cpp
#include <hip/hip_runtime.h>
#include <stdint.h>
#if defined(__HIP_DEVICE_COMPILE__)
#pragma clang attribute push (__attribute__((target("no-packed-fp32-ops"))), apply_to = function)
#endif
typedef unsigned short u16;
typedef unsigned long long u64;
typedef short s16x4 __attribute__((ext_vector_type(4)));
typedef __bf16 bf16x2 __attribute__((ext_vector_type(2)));
typedef __bf16 bf16x8 __attribute__((ext_vector_type(8)));
typedef _Float16 f16x2 __attribute__((ext_vector_type(2)));
typedef _Float16 f16x8 __attribute__((ext_vector_type(8)));
typedef float f32x2 __attribute__((ext_vector_type(2)));
typedef float f32x4 __attribute__((ext_vector_type(4)));
typedef float f32x16 __attribute__((ext_vector_type(16)));
typedef unsigned u32x2 __attribute__((ext_vector_type(2)));
typedef unsigned u32x4 __attribute__((ext_vector_type(4)));
typedef __amdgpu_buffer_rsrc_t rsrc_t;
#define LAS __attribute__((address_space(3)))
#define DI __device__ __forceinline__

DI float u2f(unsigned v){ return __builtin_bit_cast(float, v); }
DI unsigned f2u(float v){ return __builtin_bit_cast(unsigned, v); }
DI bool wave_all(bool p){ return __builtin_amdgcn_ballot_w64(!p) == 0; }
DI float bflo(unsigned v){ return u2f(v << 16); }
DI float bfhi(unsigned v){ return u2f(v & 0xffff0000u); }
DI unsigned pk_bf16(float a, float b){ f32x2 v = {a, b}; return __builtin_bit_cast(unsigned, __builtin_convertvector(v, bf16x2)); }
DI unsigned pk_f16(float a, float b){ f32x2 v = {a, b}; return __builtin_bit_cast(unsigned, __builtin_convertvector(v, f16x2)); }
DI float rbf(float f){ return bfhi(pk_bf16(f, f)); }
DI f32x4 mfma16(u32x4 a, u32x4 b, f32x4 c){ return __builtin_amdgcn_mfma_f32_16x16x32_bf16(__builtin_bit_cast(bf16x8, a), __builtin_bit_cast(bf16x8, b), c, 0, 0, 0); }
DI f32x4 mfma16h(u32x4 a, u32x4 b, f32x4 c){ return __builtin_amdgcn_mfma_f32_16x16x32_f16(__builtin_bit_cast(f16x8, a), __builtin_bit_cast(f16x8, b), c, 0, 0, 0); }
DI f32x16 mfma32h(u32x4 a, u32x4 b, f32x16 c){ return __builtin_amdgcn_mfma_f32_32x32x16_f16(__builtin_bit_cast(f16x8, a), __builtin_bit_cast(f16x8, b), c, 0, 0, 0); }
DI u32x4 lds_r128(unsigned addr){ return *(const LAS u32x4*)(uintptr_t)addr; }
DI u32x2 lds_r64(unsigned addr){ return *(const LAS u32x2*)(uintptr_t)addr; }
DI float lds_rf(unsigned addr){ return *(const LAS float*)(uintptr_t)addr; }
DI u32x2 lds_tr64(unsigned addr){ return __builtin_bit_cast(u32x2, __builtin_amdgcn_ds_read_tr16_b64_v4i16((LAS s16x4*)(uintptr_t)addr)); }
DI u32x4 lds_tr128(unsigned a0, unsigned a1){ u32x2 lo = lds_tr64(a0), hi = lds_tr64(a1); return (u32x4){lo.x, lo.y, hi.x, hi.y}; }
DI void lds_w128(unsigned addr, u32x4 v){ *(LAS u32x4*)(uintptr_t)addr = v; }
DI void lds_w64(unsigned addr, u32x2 v){ *(LAS u32x2*)(uintptr_t)addr = v; }
DI void lds_wf(unsigned addr, float v){ *(LAS float*)(uintptr_t)addr = v; }
DI u32x4 mk_rsrc(const void* p, unsigned bytes){ uint64_t a = (uint64_t)p; u32x4 r; r.x = (unsigned)a; r.y = (unsigned)(a >> 32) & 0xffffu; r.z = bytes; r.w = 0x00020000u; return r; }
DI void glds16(u32x4 rsrc, unsigned voff, unsigned soff, unsigned ldsaddr){
  asm volatile("s_mov_b32 m0, %0\n\ts_nop 0\n\tbuffer_load_dwordx4 %1, %2, %3 offen lds" :: "s"(ldsaddr), "v"(voff), "s"(rsrc), "s"(soff) : "memory");
}
DI void glds16_dev(u32x4 rsrc, unsigned voff, unsigned soff, unsigned ldsaddr){
  asm volatile("s_mov_b32 m0, %0\n\ts_nop 0\n\tbuffer_load_dwordx4 %1, %2, %3 offen sc1 lds" :: "s"(ldsaddr), "v"(voff), "s"(rsrc), "s"(soff) : "memory");
}
template <int N> DI void wait_vm(){ asm volatile("s_waitcnt vmcnt(%0)" :: "n"(N) : "memory"); }
#define BAR() asm volatile("s_barrier" ::: "memory")
#define LGKM0() asm volatile("s_waitcnt lgkmcnt(0)" ::: "memory")
#define LGKM0_BAR() asm volatile("s_waitcnt lgkmcnt(0)\n\ts_barrier" ::: "memory")
#define SCHED() __builtin_amdgcn_sched_barrier(0)
#define FENCE() asm volatile("" ::: "memory")
#define SC1 16
DI u32x4 ld128_dev(rsrc_t r, unsigned voff, unsigned soff){ return __builtin_amdgcn_raw_buffer_load_b128(r, voff, soff, SC1); }
DI u32x2 ld64_dev(rsrc_t r, unsigned voff, unsigned soff){ return __builtin_amdgcn_raw_buffer_load_b64(r, voff, soff, SC1); }
DI void st128_dev(rsrc_t r, unsigned voff, unsigned soff, u32x4 v){ __builtin_amdgcn_raw_buffer_store_b128(v, r, voff, soff, SC1); asm volatile("s_nop 1" :: "v"(v)); }
DI void st64_dev(rsrc_t r, unsigned voff, unsigned soff, u32x2 v){ __builtin_amdgcn_raw_buffer_store_b64(v, r, voff, soff, SC1); }

constexpr float LOG2E = 1.4426950408889634f, EPS = 1e-6f;
DI float phi(float t){
  const float e = __builtin_amdgcn_fmed3f(__builtin_amdgcn_exp2f(__builtin_fmaf(t, LOG2E, -EPS * LOG2E)), 0.0f, 1.0f);
  return __builtin_amdgcn_fmed3f(t, EPS, 3.0e38f) + e;
}
DI u32x4 pack8(f32x4 a, f32x4 b){ return (u32x4){pk_bf16(a[0], a[1]), pk_bf16(a[2], a[3]), pk_bf16(b[0], b[1]), pk_bf16(b[2], b[3])}; }
DI u32x4 pack8_phi(f32x4 a, f32x4 b){ return (u32x4){pk_bf16(phi(a[0]), phi(a[1])), pk_bf16(phi(a[2]), phi(a[3])), pk_bf16(phi(b[0]), phi(b[1])), pk_bf16(phi(b[2]), phi(b[3]))}; }
DI u32x4 widen16(u32x2 a, u32x2 b){
  auto x = __builtin_amdgcn_permlane16_swap(a.x, b.x, false, false), y = __builtin_amdgcn_permlane16_swap(a.y, b.y, false, false);
  return (u32x4){x[0], y[0], x[1], y[1]};
}
template <int N> struct ic { static constexpr int value = N; };
template <int B, int E, class F> DI void sfor(F&& f){ if constexpr (B < E) { f(ic<B>{}); sfor<B + 1, E>(f); } }
template <int CTRL> DI float dpp_f(float x){ return u2f(__builtin_amdgcn_update_dpp(0u, f2u(x), CTRL, 0xf, 0xf, true)); }
DI float row_sum(float x){ x += dpp_f<0x128>(x); x += dpp_f<0x124>(x); x += dpp_f<0x122>(x); x += dpp_f<0x121>(x); return x; }
DI unsigned wave_umax(unsigned x){
  auto mx = [](unsigned p, unsigned q){ return p > q ? p : q; };
  x = mx(x, __builtin_amdgcn_update_dpp(0u, x, 0x128, 0xf, 0xf, true)); x = mx(x, __builtin_amdgcn_update_dpp(0u, x, 0x124, 0xf, 0xf, true));
  x = mx(x, __builtin_amdgcn_update_dpp(0u, x, 0x122, 0xf, 0xf, true)); x = mx(x, __builtin_amdgcn_update_dpp(0u, x, 0x121, 0xf, 0xf, true));
  auto a = __builtin_amdgcn_permlane16_swap(x, x, false, false); x = mx(a[0], a[1]);
  auto b = __builtin_amdgcn_permlane32_swap(x, x, false, false); return mx(b[0], b[1]);
}
DI void lds_signal(unsigned addr, unsigned v){ LGKM0(); *(volatile LAS unsigned*)(uintptr_t)addr = v; }
template <bool SLEEP> DI void lds_wait(unsigned addr, unsigned v){
  _Pragma("unroll 1") for (unsigned sp = 0; sp < (1u << 22); ++sp) {
    if (__builtin_amdgcn_readfirstlane(*(volatile LAS unsigned*)(uintptr_t)addr) >= v) break;
    if (SLEEP) __builtin_amdgcn_s_sleep(2);
  }
  FENCE();
}
DI void lds_wait4(unsigned addr, unsigned v){
  _Pragma("unroll 1") for (unsigned sp = 0; sp < (1u << 22); ++sp) {
    const u32x4 f = *(volatile LAS u32x4*)(uintptr_t)addr;
    const unsigned m0 = f.x < f.y ? f.x : f.y, m1 = f.z < f.w ? f.z : f.w;
    if (__builtin_amdgcn_readfirstlane(m0 < m1 ? m0 : m1) >= v) break;
    __builtin_amdgcn_s_sleep(2);
  }
  FENCE();
}

#define FS 32u
constexpr unsigned WS_F1 = 4096, WS_F2 = WS_F1 + 131072, WS_F3 = WS_F2 + 131072, WS_F4 = WS_F3 + 131072, WS_GP = WS_F4 + 131072,
                   WS_PZ = WS_GP + 131072, WS_P1 = WS_PZ + 65536, WS_XT = WS_P1 + 2097152, WS_O = WS_XT + 4194304 + 4096,
                   WS_BT = WS_O + 4194304, WS_END = WS_BT + 4194304;
constexpr unsigned ST_BYTES = 40960, A_BYTES = 16384, L_UC = 40960, UC_CP = 4160, UC_STR = 16640, L_EX1 = L_UC + 4 * UC_STR, L_G = L_EX1 + 12288,
                   L_KR = 122880, KR_STR = 4608, L_FLAG = L_KR + 4576, L_CONV = L_FLAG + 16, L_BT = L_KR + 4 * KR_STR, L_EX2 = L_BT + 16384,
                   LDS_BYTES = 163840;
constexpr unsigned wo_unit(int h){
  return h < 10 ? 4096u * h : h >= 30 ? L_EX1 + 4096u * (h - 30) : (h - 10) % 5 < 4 ? L_UC + UC_STR * ((h - 10) / 5) + 4096u * ((h - 10) % 5) : L_KR + KR_STR * ((h - 10) / 5);
}
constexpr unsigned SPIN_CAP = 1u << 17;
#define KSCHED {0, 3, 7, 14, 21, 27, 33, 39, 45, 50, 56, 62, 69, 75, 81}
#define CWIN 4
#define F1AT 13
#define PSLEEP 1
#define CONVWAIT 0
#define WO0 6
#define WOPER 3
#define PF64 0
#define NOPF 0
#define WOSLEEP 3
#define CMAP 1
#define CODEPF 1
#define CODE_BYTES 28160
#define ONEPOLL 1
#define PACE 4

constexpr int kord(int j){
  constexpr int o[81] = {0, 1, 2, 3, 4, 5, 71, 6, 7, 8, 9, 10, 11, 12, 13, 14, 15, 16, 17, 18, 19, 20, 21, 22, 23, 24, 72, 25, 26, 27, 28, 73, 29, 30, 31, 32, 74, 33, 34, 35, 36, 75, 37, 38, 39, 40, 76, 41, 42, 43, 77, 44, 45, 46, 78, 47, 48, 49, 79, 50, 51, 80, 52, 53, 54, 55, 56, 57, 58, 59, 60, 61, 62, 63, 64, 65, 66, 67, 68, 69, 70};
  return o[j];
}
struct Args { const u16 *x, *w, *wo, *lar, *aim, *cr, *ci, *ldt, *dsk, *gw; u16* out; char* ws; };

#define STAMP(i)
#define STAMP_OUT(...)

struct cplx2 { f32x2 re, im; };
DI cplx2 cmul(cplx2 a, cplx2 b){ cplx2 r; r.re = a.re * b.re - a.im * b.im; r.im = a.re * b.im + a.im * b.re; return r; }

DI void poll_flags(rsrc_t rw, unsigned voff, unsigned tlo, unsigned thi){
  _Pragma("unroll 1") for (unsigned sp = 0; sp < SPIN_CAP; ++sp) {
    const u32x2 v = ld64_dev(rw, voff, 0);
    if (wave_all(v.x == tlo && v.y == thi)) break;
    __builtin_amdgcn_s_sleep(PSLEEP);
    FENCE();
  }
  FENCE();
}

__global__ __launch_bounds__(768, 3) void fused_kernel(Args a){
  extern __shared__ __attribute__((aligned(16))) char smem[];
  const char* pc; asm volatile("s_getpc_b64 %0" : "=s"(pc));
  const int tid = __builtin_amdgcn_workitem_id_x(), lane = tid & 63;
  const int wave = __builtin_amdgcn_readfirstlane(tid >> 6), lw = wave & 3;
  const int b = __builtin_amdgcn_workgroup_id_x(), xcd = b & 7, idx = b >> 3;
  const int mt = (xcd & 1) * 8 + (idx & 7), hp = (xcd >> 1) * 4 + (idx >> 3);
  const int mt2 = CMAP ? 4 * (xcd >> 1) + (idx >> 3) : 2 * xcd + (idx >> 4), nt = CMAP ? (xcd & 1) * 8 + (idx & 7) : idx & 15;
  const unsigned lds0 = (unsigned)(uintptr_t)(LAS char*)smem;
  const unsigned r8 = lane >> 3, c8 = lane & 7;

  unsigned pft = 0;
  const int pw8 = lw + (wave >= 8 ? 4 : 0);
  const char* pfw = (const char*)a.w + (size_t)(b * 24 + pw8 * 3) * 64 + 2 * 393216;
  const char* pfx = (const char*)a.x + (size_t)(b * 8 + pw8) * 2048 + 2 * 128;
  const char* pfo = (const char*)a.wo + (size_t)(b * 8 + pw8) * 1024;
  auto ahead = [&](){
    asm volatile("s_load_dword %0, %1, 0x0\n\ts_load_dword %0, %1, 0x40\n\ts_load_dword %0, %1, 0x80\n\ts_load_dword %0, %2, 0x0\n\ts_load_dword %0, %2, 0x40"
                 : "+s"(pft) : "s"(pfw), "s"(pfx) : "memory");
    pfw += 393216; pfx += 128;
  };
  auto ahead_wo = [&](){ asm volatile("s_load_dword %0, %1, 0x0\n\ts_load_dword %0, %1, 0x40" : "+s"(pft) : "s"(pfo) : "memory"); pfo += 128; };

  if (wave < 4) {
    const u32x4 rx = mk_rsrc(a.x, 2048u * 1024u * 2u), rwq = mk_rsrc(a.w, 1024u * 3072u * 2u);
    const unsigned va0 = r8 * 2048u + ((c8 ^ (r8 >> 1)) << 4), va1 = va0 ^ 64u;
    const unsigned vb0 = r8 * 6144u + ((c8 ^ (((r8 >> 1) & 1u) << 1)) << 4), vb1 = vb0 ^ 64u;
    unsigned oa = (unsigned)(mt * 128 + lw * 32) * 2048u, ob = (unsigned)(lw * 16) * 6144u + (unsigned)hp * 128u;
    auto issue = [&](unsigned buf){
      const unsigned la = lds0 + buf + lw * 4096u, lb = lds0 + buf + A_BYTES + lw * 2048u;
      glds16(rx, va0, oa, la);
      glds16(rwq, vb0, ob ^ 512u, lb); glds16(rwq, vb0, ob + 2048u, lb + 8192u); glds16(rwq, vb0, ob + 4096u, lb + 16384u);
      glds16(rx, va1, oa + 16384u, la + 1024u);
      glds16(rwq, vb1, (ob ^ 512u) + 49152u, lb + 1024u); glds16(rwq, vb1, ob + 51200u, lb + 9216u); glds16(rwq, vb1, ob + 53248u, lb + 17408u);
      glds16(rx, va0, oa + 32768u, la + 2048u);
      glds16(rx, va1, oa + 49152u, la + 3072u);
      oa += 128u; ob += 64u * 6144u;
    };
    const rsrc_t rwo = __builtin_amdgcn_make_buffer_rsrc((void*)a.wo, 0, 1024u * 1024u * 2u, 0x00020000);
    const unsigned vwo = r8 * 2048u + ((c8 ^ (((r8 >> 1) & 3u) << 1)) << 4) + nt * 128u;
    const int hb = lw < 2 ? 8 + lw : 28 + lw;
    u32x4 wr_[32];
    auto ldwo = [&](int i0, int i1){
      #pragma unroll
      for (int i = i0; i < i1; ++i) {
        const int h = i < 4 ? lw : i < 8 ? lw + 4 : i < 12 ? hb : 10 + 5 * lw + ((i - 12) >> 2);
        wr_[i] = __builtin_amdgcn_raw_buffer_load_b128(rwo, vwo, (unsigned)(h * 32 + (i & 3) * 8) * 2048u, 0);
        FENCE(); __builtin_amdgcn_s_sleep(WOSLEEP); FENCE();
      }
    };
#define LT(x, i)
    issue(0);
    ahead();
    unsigned buf = ST_BYTES;
    for (int s = 1; s < 16; ++s) {
      issue(buf);
      if (s < 14) ahead();
      if (s >= 5 && s < 13) ahead_wo();
      buf = (buf == 2 * ST_BYTES) ? 0 : buf + ST_BYTES;
      wait_vm<10>();
      LT(tsa, s - 1)
      BAR();
      LT(tsb, s - 1)
    }
    asm volatile("s_waitcnt vmcnt(0) lgkmcnt(0)" :: "s"(pft) : "memory");
    LT(tsa, 15)
    BAR();
    LT(tsb, 15) LT(tsa, 16)
    BAR();
    LT(tsb, 16)
    ldwo(0, 32);
    FENCE();
    const unsigned wl = lds0 + lane * 16u;
    #pragma unroll
    for (int i = 0; i < 8; ++i) lds_w128(wl + (i < 4 ? lw : lw + 4) * 4096u + (i & 3) * 1024u, wr_[i]);
    if (lw < 2) {
      #pragma unroll
      for (int i = 8; i < 12; ++i) lds_w128(wl + hb * 4096u + (i & 3) * 1024u, wr_[i]);
    }
    lds_wait<true>(lds0 + L_CONV + lw * 4u, 2u);
    #pragma unroll
    for (int i = 12; i < 32; ++i) lds_w128(wl + (i < 28 ? L_UC + lw * UC_STR + (i - 12) * 1024u : L_KR + lw * KR_STR + (i - 28) * 1024u), wr_[i]);
    if (lw >= 2) {
      lds_wait4(lds0 + L_FLAG, 2u);
      #pragma unroll
      for (int i = 8; i < 12; ++i) lds_w128(wl + L_EX1 + (lw - 2) * 4096u + (i & 3) * 1024u, wr_[i]);
    }
    LGKM0();
    return;
  }

  const rsrc_t rw = __builtin_amdgcn_make_buffer_rsrc(a.ws, 0, WS_END, 0x00020000);
  const int g = lane >> 4, l16 = lane & 15, q4 = l16 >> 2, p4 = lane & 3;
  auto load_tag = [&](){ u64 t; asm volatile("s_load_dwordx2 %0, %1, 0x0\n\ts_waitcnt lgkmcnt(0)" : "=s"(t) : "s"(a.ws) : "memory"); return t + 1ull; };
  STAMP(0);
  if (wave >= 8) {
    const int d = b * 4 + lw;
    const unsigned kreg = lds0 + L_KR + lw * KR_STR, ureg = lds0 + L_UC + lw * UC_STR;
    unsigned p_lar, p_aim, p_cr, p_ci, p_ldt, p_dsk;
    {
      const unsigned po = (unsigned)(d * 128 + 2 * lane) * 2u, dd = (unsigned)d * 2u;
      asm volatile("global_load_dword %0, %6, %8\n\tglobal_load_dword %1, %6, %9\n\tglobal_load_dword %2, %6, %10\n\tglobal_load_dword %3, %6, %11\n\t"
                   "global_load_ushort %4, %7, %12\n\tglobal_load_ushort %5, %7, %13"
                   : "=&v"(p_lar), "=&v"(p_aim), "=&v"(p_cr), "=&v"(p_ci), "=&v"(p_ldt), "=&v"(p_dsk)
                   : "v"(po), "v"(dd), "s"(a.lar), "s"(a.aim), "s"(a.cr), "s"(a.ci), "s"(a.ldt), "s"(a.dsk) : "memory");
    }
    {
      const char* p = pc + (size_t)((idx * 4 + lw) % (CODE_BYTES / 256)) * 256;
      asm volatile("s_load_dword %0, %1, 0x0\n\ts_load_dword %0, %1, 0x40\n\ts_load_dword %0, %1, 0x80\n\ts_load_dword %0, %1, 0xc0" : "+s"(pft) : "s"(p) : "memory");
    }
    u32x4 gw0, gw1;
    {
      const unsigned vx = r8 * 2048u + ((c8 ^ (((r8 >> 1) & 1u) << 1)) << 4), ox = (unsigned)(mt * 128 + lw * 32) * 2048u + hp * 128u;
      const u32x4 rx = mk_rsrc(a.x, 2048u * 1024u * 2u);
      glds16(rx, vx, ox, kreg); glds16(rx, vx ^ 64u, ox + 16384u, kreg + 1024u); glds16(rx, vx, ox + 32768u, kreg + 2048u); glds16(rx, vx ^ 64u, ox + 49152u, kreg + 3072u);
    }
    ahead(); ahead();
    asm volatile("s_barrier\n\ts_waitcnt vmcnt(4)"
                 : "+v"(p_lar), "+v"(p_aim), "+v"(p_cr), "+v"(p_ci), "+v"(p_ldt), "+v"(p_dsk) :: "memory");
    p_dsk = __builtin_amdgcn_readfirstlane(p_dsk);
    unsigned tlo, thi;

    float dt, unscale; f32x2 a2, ph; cplx2 C, w1, w2, w4, w8, w16, w256, v, c256, hold; unsigned mu, l01, l23;
    u32x4 qf[8], pf[8], sq[4][4][2], sel[2];
    f32x4 k1 = {0, 0, 0, 0}, k2 = {0, 0, 0, 0}, k3 = {0, 0, 0, 0};
    auto put = [&](int t, int j){
      if (j & 1) {
        unsigned x0 = pk_f16(hold.re[0], v.re[0]), x1 = pk_f16(hold.im[0], v.im[0]), x2 = pk_f16(hold.re[1], v.re[1]), x3 = pk_f16(hold.im[1], v.im[1]);
        asm volatile("" : "+v"(x0), "+v"(x1), "+v"(x2), "+v"(x3));
        sq[t][0][j >> 3][(j >> 1) & 3] = x0; sq[t][1][j >> 3][(j >> 1) & 3] = x1; sq[t][2][j >> 3][(j >> 1) & 3] = x2; sq[t][3][j >> 3][(j >> 1) & 3] = x3;
      } else hold = v;
    };
    auto pin = [&](cplx2& x){ asm volatile("" : "+v"(x.re), "+v"(x.im)); };
    auto putmul = [&](int t, int j, const cplx2& x, const cplx2& w){
      const f32x2 t0 = x.im * w.im, t1 = x.im * w.re; SCHED();
      put(t, j); SCHED();
      cplx2 r; r.im = x.re * w.im + t1; SCHED();
      r.re = x.re * w.re - t0; SCHED();
      v = r;
    };
    auto pw = [&](cplx2& r, int m, float p){
      const float mag = __builtin_amdgcn_exp2f(a2[m] * p), rev = __builtin_amdgcn_fractf(ph[m] * p);
      r.re[m] = mag * __builtin_amdgcn_cosf(rev); r.im[m] = mag * __builtin_amdgcn_sinf(rev);
    };
    auto mksel = [&](){
      const int e0 = (lane & 31) - 16 * (lane >> 5);
      const unsigned one = (e0 & 1) ? 0x3c000000u : 0x00003c00u;
      #pragma unroll
      for (int i = 0; i < 8; ++i) sel[i >> 2][i & 3] = (e0 >> 1) == i ? one : 0u;
      asm volatile("" : "+v"(sel[0]), "+v"(sel[1]));
    };
    auto tp = [&](u32x4 (&f)[8], int t, int c){
      f32x16 z;
      #pragma unroll
      for (int i = 0; i < 16; ++i) z[i] = 0.f;
      z = mfma32h(sq[t][c][0], sel[0], z); z = mfma32h(sq[t][c][1], sel[1], z);
      #pragma unroll
      for (int i = 0; i < 8; ++i) f[2 * c + (i >> 2)][i & 3] = pk_f16(z[2 * i], z[2 * i + 1]);
    };
    auto mmk = [&](f32x4& k){
      #pragma unroll
      for (int ks = 0; ks < 8; ++ks) k = mfma16h(pf[ks], qf[ks], k);
    };
    auto kstep = [&](auto IC){
      constexpr int I = decltype(IC)::value;
      if constexpr (I == 0) {
        dt = rbf(__builtin_amdgcn_exp2f(bflo(p_ldt) * LOG2E));
        a2[0] = -rbf(__builtin_amdgcn_exp2f(bflo(p_lar) * LOG2E)) * (dt * LOG2E);
      } else if constexpr (I == 1) {
        a2[1] = -rbf(__builtin_amdgcn_exp2f(bfhi(p_lar) * LOG2E)) * (dt * LOG2E);
        ph[0] = bflo(p_aim) * (dt * 0.15915494309189535f); ph[1] = bfhi(p_aim) * (dt * 0.15915494309189535f);
      } else if constexpr (I == 2) { pw(w16, 0, 16.0f); }
      else if constexpr (I == 3) { pw(w16, 1, 16.0f); w16.im = -w16.im; pin(w16); }
      else if constexpr (I == 4) {
        const unsigned m1 = (p_cr & 0x7fff7fffu), m2 = (p_ci & 0x7fff7fffu);
        mu = (m1 << 16) > m1 ? (m1 << 16) : m1; const unsigned t = (m2 << 16) > m2 ? (m2 << 16) : m2; mu = wave_umax(mu > t ? mu : t);
        asm volatile("" : "+v"(mu));
      } else if constexpr (I == 5) {
        int e = __builtin_amdgcn_frexp_expf(u2f(mu & 0xffff0000u)); e = e < -100 ? -100 : (e > 100 ? 100 : e);
        const float sc = __builtin_ldexpf(1.0f, 14 - e);
        C.re = (f32x2){bflo(p_cr) * sc, bfhi(p_cr) * sc}; C.im = (f32x2){bflo(p_ci) * sc, bfhi(p_ci) * sc};
        unscale = u2f(__builtin_amdgcn_readfirstlane(f2u(2.0f * dt * __builtin_ldexpf(1.0f, e - 28))));
        pin(C);
        v.re = (f32x2){16384.f, 16384.f}; v.im = (f32x2){0.f, 0.f};
      } else if constexpr (I < 22) { if constexpr (I < 21) putmul(0, I - 6, v, w16); else { put(0, 15); v = C; } }
      else if constexpr (I == 22) { pw(w1, 0, 1.0f); }
      else if constexpr (I == 23) { pw(w1, 1, 1.0f); pin(w1); }
      else if constexpr (I < 40) { if constexpr (I < 39) putmul(1, I - 24, v, w1); else put(1, 15); }
      else if constexpr (I == 40) { pw(w256, 0, 256.0f); }
      else if constexpr (I == 41) { pw(w256, 1, 256.0f); pin(w256); }
      else if constexpr (I == 42) { w2 = cmul(w1, w1); pin(w2); }
      else if constexpr (I == 43) { w4 = cmul(w2, w2); pin(w4); }
      else if constexpr (I == 44) { c256 = cmul(C, w256); pin(c256); v = c256; }
      else if constexpr (I < 61) {
        constexpr int j = I - 45;
        if constexpr (j == 7 || j == 11 || j == 15) { putmul(2, j, c256, w256); c256 = v; } else putmul(2, j, v, j < 7 ? w2 : w4);
      } else if constexpr (I == 61) { w8 = cmul(w4, w4); pin(w8); }
      else if constexpr (I == 71) mksel();
      else if constexpr (I >= 72) {
        __builtin_amdgcn_s_setprio(3);
        if constexpr (I < 76) { tp(qf, 0, I - 72); asm volatile("" : "+v"(qf[2 * (I - 72)]), "+v"(qf[2 * (I - 72) + 1])); }
        else if constexpr (I < 80) { tp(pf, 1, I - 76); asm volatile("" : "+v"(pf[2 * (I - 76)]), "+v"(pf[2 * (I - 76) + 1])); }
        else { mmk(k1); asm volatile("" : "+v"(k1)); }
        __builtin_amdgcn_s_setprio(0);
      } else {
        constexpr int j = I - 62;
        if constexpr (j == 8) { l01 = pk_f16(v.re[0], v.im[0]); l23 = pk_f16(v.re[1], v.im[1]); asm volatile("" : "+v"(l01), "+v"(l23)); } else if constexpr (j & 1) { putmul(3, j, c256, w256); c256 = v; } else putmul(3, j, v, w8);
      }
    };
    unsigned xta = kreg + (unsigned)(8 * g + q4) * 128u + ((unsigned)(((q4 >> 1) & 1) | ((g & 1) << 1)) << 5) + 8u * p4;
    unsigned xvo = WS_XT + (unsigned)(hp * 64 + l16) * 4096u + (unsigned)(mt * 128 + 32 * lw + 8 * g) * 2u;
    asm volatile("" : "+v"(xta), "+v"(xvo));
    u32x2 xlo, xhi, f1; u32x4 xt, um[4]; u32x2 uh[4];
    const unsigned f1o = WS_F1 + (unsigned)((d >> 6) * 64 + lane) * FS;
    auto ldu = [&](){
      const unsigned vo = WS_XT + (unsigned)d * 4096u + lane * 16u;
      #pragma unroll
      for (int i = 0; i < 4; ++i) { um[i] = ld128_dev(rw, vo + i * 1024u, 0); uh[i] = ld64_dev(rw, vo + i * 1024u + 16u, 0); }
    };
    sfor<0, 14>([&](auto SC){
      constexpr int S = decltype(SC)::value;
      constexpr int first[15] = KSCHED;
      if constexpr (S >= 2 && S < 10) {
        const unsigned p = xta ^ (((S - 2) >> 1) << 5);
        asm volatile("ds_read_b64_tr_b16 %0, %2\n\tds_read_b64_tr_b16 %1, %2 offset:512\n\ts_waitcnt lgkmcnt(0)" : "=&v"(xlo), "=&v"(xhi) : "v"(p) : "memory");
        xt = (u32x4){xlo.x, xlo.y, xhi.x, xhi.y};
      }
      if constexpr (S == 10) { const u64 tag = load_tag(); tlo = (unsigned)tag; thi = (unsigned)(tag >> 32); }
      if constexpr (S < 12 && S != 1) ahead();
      if constexpr (S == 2) ahead();
      if constexpr (S >= 3 && S < 11) ahead_wo();
      sfor<first[S], first[S + 1]>([&](auto IC){ constexpr int J = decltype(IC)::value; kstep(ic<kord(J)>{}); SCHED(); });
      if constexpr (S > 0 && first[S + 1] < 81) pin(v);
      if constexpr (S == 0) { gw0 = *(const u32x4*)(a.gw + hp * 64 + 8 * g); gw1 = *(const u32x4*)(a.gw + hp * 64 + 32 + 8 * g); }
      if constexpr (S == 1) {
        wait_vm<0>();
        const unsigned fr = (unsigned)(((l16 >> 1) & 1) | (((l16 >> 3) & 1) << 1)) << 1;
        const unsigned ga = kreg + l16 * 128u + ((g ^ fr) << 4);
        f32x4 g0 = {0, 0, 0, 0}, g1 = {0, 0, 0, 0};
        const u32x4 x0 = lds_r128(ga), x1 = lds_r128(ga ^ 64u), x2 = lds_r128(ga + 2048u), x3 = lds_r128((ga ^ 64u) + 2048u);
        __builtin_amdgcn_s_setprio(3);
        g0 = mfma16(x0, gw0, g0); g1 = mfma16(x2, gw0, g1); g0 = mfma16(x1, gw1, g0); g1 = mfma16(x3, gw1, g1);
        __builtin_amdgcn_s_setprio(0);
        if (l16 < 2) st128_dev(rw, WS_GP + (unsigned)((mt * 16 + hp) * 128 + 32 * lw + 16 * l16 + 4 * g) * 4u, 0, __builtin_bit_cast(u32x4, l16 ? g1 : g0));
      }
      if constexpr (S >= 2 && S < 10) { if ((lw & 1) == (S & 1)) st128_dev(rw, xvo, ((S - 2) >> 1) * 65536u, xt); }
      if constexpr (S == 11) { wait_vm<0>(); if (lane == 0) st64_dev(rw, WS_F1 + (unsigned)((hp * 16 + mt) * 4 + lw) * FS, 0, (u32x2){tlo, thi}); }
      if constexpr (S == 13) f1 = ld64_dev(rw, f1o, 0);
      SCHED();
      BAR(); SCHED();
    });
    asm volatile("s_waitcnt lgkmcnt(0)" :: "s"(pft) : "memory");
    const bool have_u = wave_all(f1.x == tlo && f1.y == thi);
    if (have_u) ldu();
    SCHED(); BAR(); SCHED();
    auto copies = [&](int i0, int i1){
      if (i0 == 0) {
        const unsigned h0 = __builtin_amdgcn_readfirstlane(um[0].x), h1 = __builtin_amdgcn_readfirstlane(um[0].y);
        const int sg = lane >> 1;
        u32x4 lead = {0, 0, 0, 0};
        if (lane & 1) { lead.z = sg == 3 ? h0 << 16 : 0u; lead.w = sg == 1 ? h0 << 16 : sg == 2 ? h0 : sg == 3 ? (h0 >> 16) | (h1 << 16) : 0u; }
        if (lane < 8) lds_w128(ureg + sg * UC_CP + (lane & 1) * 16u, lead);
      }
      const unsigned uw = ureg + 32u + lane * 16u;
      #pragma unroll
      for (int i = i0; i < i1; ++i) {
        const unsigned w0 = um[i].x, w1 = um[i].y, w2 = um[i].z, w3 = um[i].w, w4 = uh[i].x, w5 = uh[i].y;
        const u32x4 c1 = {__builtin_amdgcn_alignbit(w1, w0, 16), __builtin_amdgcn_alignbit(w2, w1, 16), __builtin_amdgcn_alignbit(w3, w2, 16), __builtin_amdgcn_alignbit(w4, w3, 16)};
        lds_w128(uw + i * 1024u, um[i]); lds_w128(uw + UC_CP + i * 1024u, c1);
        lds_w128(uw + 2u * UC_CP + i * 1024u, (u32x4){w1, w2, w3, w4});
        lds_w128(uw + 3u * UC_CP + i * 1024u, (u32x4){c1.y, c1.z, c1.w, __builtin_amdgcn_alignbit(w5, w4, 16)});
      }
      FENCE();
    };
    __builtin_amdgcn_s_setprio(1);
    SCHED(); BAR(); SCHED();
    STAMP(1);
    if (!have_u) { poll_flags(rw, f1o, tlo, thi); ldu(); }
    {
      #pragma unroll
      for (int c = 0; c < 4; ++c) tp(pf, 2, c);
      mmk(k2);
      SCHED();
      STAMP(12);
      copies(0, 4);
      STAMP(13);
      #pragma unroll
      for (int c = 0; c < 4; ++c) { sq[3][c][1] = (u32x4){(c < 2 ? l01 : l23) >> ((c & 1) * 16), 0, 0, 0}; tp(pf, 3, c); }
      mmk(k3);
      k1 *= unscale; k2 *= unscale; k3 *= unscale;
      if (lane == 0) k1[0] += bflo(p_dsk);
      const unsigned T = lds0 + L_BT + lw * 2048u;
      lds_w128(T + (unsigned)(g < 2 ? 8 * l16 + 4 * g : 128 + 64 * (g - 2) + 4 * l16) * 4u, __builtin_bit_cast(u32x4, k2));
      if (g < 2) {
        lds_w64(T + (unsigned)(256 + 64 * g + 2 * l16) * 4u, (u32x2){f2u(k3[0]), f2u(k3[1])});
        lds_w64(T + (unsigned)(256 + 64 * g + 32 + 2 * l16) * 4u, (u32x2){f2u(k3[2]), f2u(k3[3])});
      }
      if (lane == 32) lds_wf(T + 384u * 4u, k3[0]);
      FENCE();
      const unsigned c = lane & 31;
      const f32x4 eb = __builtin_bit_cast(f32x4, lds_r128(T + c * 16u)); const float e4 = lds_rf(T + c * 16u + 16u);
      const float c0 = lds_rf(T + (128u + 2u * lane) * 4u), c1 = lds_rf(T + (129u + 2u * lane) * 4u), c2 = lds_rf(T + (130u + 2u * lane) * 4u);
      const float d0 = lds_rf(T + (256u + 2u * lane) * 4u), d1 = lds_rf(T + (257u + 2u * lane) * 4u), d2 = lds_rf(T + (258u + 2u * lane) * 4u);
      FENCE();
      lds_w64(kreg + (unsigned)(2044 - 16 * l16 - 4 * g) * 2u, (u32x2){pk_bf16(k1[3], k1[2]), pk_bf16(k1[1], k1[0])});
      if (lane < 30) lds_w128(kreg + 4096u + lane * 16u, (u32x4){0, 0, 0, 0});
      lds_w128(kreg + (1784u - 8u * c) * 2u, (u32x4){pk_bf16(e4, eb[3]), pk_bf16(eb[2], eb[2]), pk_bf16(eb[2], eb[1]), pk_bf16(eb[0], eb[0])});
      lds_w128(kreg + (1528u - 8u * lane) * 2u, (u32x4){pk_bf16(c2, c2), pk_bf16(c1, c1), pk_bf16(c1, c0), pk_bf16(c0, c0)});
      const unsigned aa = pk_bf16(d0, d0), bb = pk_bf16(d1, d1), ee = pk_bf16(d2, d2);
      lds_w128(kreg + (1016u - 16u * lane) * 2u, (u32x4){bb, pk_bf16(d1, d0), aa, aa});
      lds_w128(kreg + (1008u - 16u * lane) * 2u, (u32x4){ee, ee, bb, bb});
    }
    STAMP(15);
    STAMP(2);
    {
      f32x4 acc[8];
      #pragma unroll
      for (int c = 0; c < 8; ++c) acc[c] = (f32x4){0, 0, 0, 0};
      unsigned aA = ureg + (unsigned)((1 + q4) & 3) * UC_CP + (unsigned)(8 * g + 4 * p4 + (q4 == 3 ? 4 : 0)) * 2u;
      unsigned aB = kreg + (unsigned)(2032 - 256 * 7 - 16 * l16 + 8 * g) * 2u;
      u32x4 fa[2][8], fb[2][8];
      auto ldf = [&](int set, int r){
        #pragma unroll
        for (int q = 0; q < 8; ++q) {
          fb[set][q] = lds_r128(aB + r * 64u + (7 - q) * 512u);
          fa[set][q] = lds_tr128(aA + r * 64u + q * 512u, aA + r * 64u + q * 512u + 8u);
        }
      };
      auto mm = [&](int set){
        #pragma unroll
        for (int q = 0; q < 8; ++q)
          #pragma unroll
          for (int c = q; c < 8; ++c) acc[c] = mfma16(fa[set][q], fb[set][c - q], acc[c]);
      };
      auto ilv = [&](){
        #pragma unroll
        for (int i = 0; i < 24; ++i) { __builtin_amdgcn_sched_group_barrier(0x008, 1, 0); __builtin_amdgcn_sched_group_barrier(0x100, 1, 0); }
        __builtin_amdgcn_sched_group_barrier(0x008, 12, 0);
      };
      ldf(0, 0);
      *(volatile LAS unsigned*)(uintptr_t)(lds0 + L_CONV + lw * 4u) = 1u;
      __builtin_amdgcn_s_setprio(1);
      STAMP(5);
      for (int r = 0; r < 8; r += 2) {
        asm volatile("" : "+v"(aA), "+v"(aB));
        SCHED();
        ldf(1, 1); mm(0); ilv(); SCHED();
        ldf(0, 2); mm(1); ilv(); SCHED();
        aA += 128u; aB += 128u;
      }
      SCHED();
      lds_signal(lds0 + L_CONV + lw * 4u, 2u);
      const unsigned vo = WS_BT + (unsigned)d * 4096u + (unsigned)(256 * (g & 1) + 16 * l16 + 8 * (g >> 1)) * 2u;
      #pragma unroll
      for (int c = 0; c < 8; c += 2)
        st128_dev(rw, vo + c * 512u, 0, widen16((u32x2){pk_bf16(acc[c][0], acc[c][1]), pk_bf16(acc[c][2], acc[c][3])},
                                                (u32x2){pk_bf16(acc[c + 1][0], acc[c + 1][1]), pk_bf16(acc[c + 1][2], acc[c + 1][3])}));
    }
    STAMP(3);
    wait_vm<0>();
    if (lane == 0) st64_dev(rw, WS_F4 + (unsigned)d * FS, 0, (u32x2){tlo, thi});
    STAMP(4);
    BAR();
    if (lw == 1) {
      poll_flags(rw, WS_F1 + (unsigned)(((lane >> 2) * 16 + mt2) * 4 + (lane & 3)) * FS, tlo, thi);
      u32x2 gp[16];
      #pragma unroll
      for (int i = 0; i < 16; ++i) gp[i] = ld64_dev(rw, WS_GP + (unsigned)(mt2 * 16 * 128 + 2 * lane) * 4u + i * 512u, 0);
      float s0 = 0.f, s1 = 0.f;
      #pragma unroll
      for (int i = 0; i < 16; ++i) { s0 += u2f(gp[i].x); s1 += u2f(gp[i].y); }
      auto sig = [](float s){ return rbf(__builtin_amdgcn_rcpf(1.0f + __builtin_amdgcn_exp2f(-rbf(s) * LOG2E))); };
      lds_w64(lds0 + L_G + lane * 8u, (u32x2){f2u(sig(s0)), f2u(sig(s1))});
    } else if (lw >= 2) {
      poll_flags(rw, WS_F4 + (unsigned)(nt * 64 + (lw - 2) * 32 + (lane & 31)) * FS, tlo, thi);
      const u32x4 rws = mk_rsrc(a.ws, WS_END);
      const unsigned vo0 = (unsigned)g * 4096u + ((unsigned)(l16 ^ (g << 1)) << 4), vo1 = vo0 ^ 128u;
      const unsigned so = WS_BT + (unsigned)(nt * 64 + (lw - 2) * 32) * 4096u + (unsigned)mt2 * 256u;
      #pragma unroll
      for (int i = 0; i < 8; ++i) glds16_dev(rws, (i & 1) ? vo1 : vo0, so + i * 16384u, lds0 + L_BT + (lw - 2) * 8192u + i * 1024u);
      wait_vm<0>();
    }
    STAMP(6);
    STAMP_OUT(0, 1, 12, 13, 15, 2, 5, 3, 4, 6);
    LGKM0();
    return;
  }
  {
    const int wr = lw >> 1, wc = lw & 1;
    const unsigned aA = lds0 + (unsigned)(wr * 64 + l16) * 128u + ((unsigned)(g ^ ((l16 >> 1) & 7)) << 4);
    const unsigned aB0 = lds0 + A_BYTES + (unsigned)(8 * g + q4) * 128u + ((unsigned)((2 * wc) ^ (((q4 >> 1) & 1) | ((g & 1) << 1))) << 5) + 8u * p4;
    f32x4 acc[3][4][2];
    #pragma unroll
    for (int i = 0; i < 3; ++i)
      #pragma unroll
      for (int j = 0; j < 4; ++j) { const float z = i < 2 ? EPS : 0.f; acc[i][j][0] = (f32x4){z, z, z, z}; acc[i][j][1] = (f32x4){z, z, z, z}; asm volatile("" : "+v"(acc[i][j][0]), "+v"(acc[i][j][1])); }
    u32x4 fa[2][4], fb[6];
    unsigned pa0, pa1, pb0, pb1;
    auto setbuf = [&](unsigned buf){ pa0 = aA + buf; pa1 = pa0 ^ 64u; pb0 = aB0 + buf; pb1 = pb0 ^ 32u; asm volatile("" : "+v"(pa0), "+v"(pa1), "+v"(pb0), "+v"(pb1)); };
    auto lda = [&](int set, int ks){
      #pragma unroll
      for (int rt = 0; rt < 4; ++rt) fa[set][rt] = lds_r128((ks ? pa1 : pa0) + rt * 2048u);
    };
    auto ldb = [&](int sg, int ks){
      const unsigned o = sg * 8192u + ks * 4096u;
      fb[2 * sg] = lds_tr128(pb0 + o, pb0 + o + 512u);
      fb[2 * sg + 1] = lds_tr128(pb1 + o, pb1 + o + 512u);
    };
    auto mm = [&](int set, int sg){
      #pragma unroll
      for (int ct = 0; ct < 2; ++ct)
        #pragma unroll
        for (int rt = 0; rt < 4; ++rt)
          acc[sg][rt][ct] = sg == 0 ? mfma16(fb[ct], fa[set][rt], acc[sg][rt][ct]) : mfma16(fa[set][rt], fb[2 * sg + ct], acc[sg][rt][ct]);
    };
    auto ilv = [&](int n){
      #pragma unroll
      for (int i = 0; i < 8; ++i) { __builtin_amdgcn_sched_group_barrier(0x008, 1, 0); if (i < n) __builtin_amdgcn_sched_group_barrier(0x100, 1, 0); }
      SCHED();
    };
    if (lane < 2) *(volatile LAS unsigned*)(uintptr_t)(lds0 + L_FLAG + lw * 4u + lane * 16u) = 0u;
    setbuf(0);
    const u64 tag = load_tag();
    BAR();
    const unsigned tlo = (unsigned)tag, thi = (unsigned)(tag >> 32);
    STAMP(1);
    lda(0, 0); ldb(0, 0); ldb(1, 0);
    unsigned buf = 0;
    for (int s = 0; s < 16; ++s) {
      SCHED();
      ldb(2, 0); mm(0, 0); ilv(4);
      lda(1, 1); ldb(0, 1); mm(0, 1); ilv(8);
      ldb(1, 1); mm(0, 2); ilv(4);
      ldb(2, 1); mm(1, 0); ilv(4);
      buf = (buf == 2 * ST_BYTES) ? 0 : buf + ST_BYTES;
      setbuf(buf);
      LGKM0_BAR(); SCHED();
      lda(0, 0); ldb(0, 0); mm(1, 1); ilv(8);
      ldb(1, 0); mm(1, 2); ilv(4);
    }
    SCHED();
    __builtin_amdgcn_s_setprio(2);
    STAMP(2);
    const int hk = (hp * 2 + wc) * 2 + wr, hq = hk ^ 16;
    {
      f32x4 T[2][2], Z[2];
      #pragma unroll
      for (int i = 0; i < 2; ++i) { T[i][0] = (f32x4){0, 0, 0, 0}; T[i][1] = (f32x4){0, 0, 0, 0}; Z[i] = (f32x4){0, 0, 0, 0}; }
      const u32x4 ones = {0x3f803f80u, 0x3f803f80u, 0x3f803f80u, 0x3f803f80u};
      #pragma unroll
      for (int rp = 0; rp < 2; ++rp) {
        u32x4 pk[2], vv[2];
        #pragma unroll
        for (int t = 0; t < 2; ++t) { pk[t] = pack8_phi(acc[1][2 * rp][t], acc[1][2 * rp + 1][t]); vv[t] = pack8(acc[2][2 * rp][t], acc[2][2 * rp + 1][t]); }
        #pragma unroll
        for (int dt = 0; dt < 2; ++dt) { T[dt][0] = mfma16(pk[dt], vv[0], T[dt][0]); T[dt][1] = mfma16(pk[dt], vv[1], T[dt][1]); Z[dt] = mfma16(pk[dt], ones, Z[dt]); }
      }
      const unsigned exw = lds0 + L_EX1 + lw * 3072u + lane * 16u, exr = lds0 + L_EX1 + (lw ^ 2) * 3072u + lane * 16u;
      lds_w128(exw, __builtin_bit_cast(u32x4, wr ? T[0][0] : T[0][1])); lds_w128(exw + 1024u, __builtin_bit_cast(u32x4, wr ? T[1][0] : T[1][1]));
      lds_w128(exw + 2048u, __builtin_bit_cast(u32x4, wr ? Z[0] : Z[1]));
      lds_signal(lds0 + L_FLAG + lw * 4u, 1u); lds_wait<false>(lds0 + L_FLAG + (lw ^ 2) * 4u, 1u);
      const f32x4 t0 = (wr ? T[0][1] : T[0][0]) + __builtin_bit_cast(f32x4, lds_r128(exr));
      const f32x4 t1 = (wr ? T[1][1] : T[1][0]) + __builtin_bit_cast(f32x4, lds_r128(exr + 1024u));
      const f32x4 tz = (wr ? Z[1] : Z[0]) + __builtin_bit_cast(f32x4, lds_r128(exr + 2048u));
      st128_dev(rw, lane * 16u, WS_P1 + (unsigned)(hk * 16 + mt) * 1024u, pack8(t0, t1));
      if (l16 == 0) st128_dev(rw, g * 16u, WS_PZ + (unsigned)(hk * 16 + mt) * 64u, __builtin_bit_cast(u32x4, tz));
    }
    SCHED();
    STAMP(3);
    u32x4 pq[4];
    #pragma unroll
    for (int st = 0; st < 4; ++st) {
      pq[st] = pack8_phi(acc[0][st][0], acc[0][st][1]);
      asm volatile("" : "+v"(pq[st]));
      if (st == 1) {
        SCHED(); wait_vm<0>();
        if (lane == 0) st64_dev(rw, WS_F2 + (unsigned)(hk * 16 + mt) * FS, 0, (u32x2){tlo, thi});
        SCHED();
      }
    }
    SCHED();
    u32x2 id = {0, 0};
    if (q4 == g) { const unsigned one = (p4 & 1) ? 0x3f800000u : 0x00003f80u; if (p4 & 2) id.y = one; else id.x = one; }
    asm volatile("" : "+v"(id));
    STAMP(11);
    poll_flags(rw, WS_F2 + (unsigned)(hq * 16 + l16) * FS, tlo, thi);
    STAMP(4);
    u32x4 zf, kvf[2];
    {
      const unsigned base = WS_P1 + (unsigned)(hq * 16) * 1024u;
      const f32x4 zp = __builtin_bit_cast(f32x4, ld128_dev(rw, l16 * 64u + g * 16u, WS_PZ + (unsigned)(hq * 16) * 64u));
      u32x4 v[16];
      #pragma unroll
      for (int i = 0; i < 16; ++i) v[i] = ld128_dev(rw, lane * 16u, base + i * 1024u);
      SCHED();
      const u32x2 zh = {pk_bf16(row_sum(zp[0]), row_sum(zp[1])), pk_bf16(row_sum(zp[2]), row_sum(zp[3]))};
      f32x4 s0 = {0, 0, 0, 0}, s1 = {0, 0, 0, 0};
      #pragma unroll
      for (int i = 0; i < 16; ++i) { s0 = mfma16((u32x4){id.x, id.y, 0, 0}, v[i], s0); s1 = mfma16((u32x4){0, 0, id.x, id.y}, v[i], s1); }
      const u32x4 mine = pack8(s0, s1);
      const unsigned exw = lds0 + L_EX2 + lw * 1536u, exr = lds0 + L_EX2 + (lw ^ 2) * 1536u;
      lds_w128(exw + lane * 16u, mine); lds_w64(exw + 1024u + lane * 8u, zh);
      lds_signal(lds0 + L_FLAG + lw * 4u, 2u); lds_wait<false>(lds0 + L_FLAG + (lw ^ 2) * 4u, 2u);
      const u32x4 other = lds_r128(exr + lane * 16u); const u32x2 zo = lds_r64(exr + 1024u + lane * 8u);
      kvf[0] = wr ? other : mine; kvf[1] = wr ? mine : other;
      zf = wr ? (u32x4){zo.x, zo.y, zh.x, zh.y} : (u32x4){zh.x, zh.y, zo.x, zo.y};
    }
    STAMP(5);
    #pragma unroll
    for (int st = 0; st < 4; ++st) {
      const f32x4 zero = {0, 0, 0, 0};
      const f32x4 o0 = mfma16(kvf[0], pq[st], zero), o1 = mfma16(kvf[1], pq[st], zero), dn = mfma16(zf, pq[st], zero);
      const float inv = __builtin_amdgcn_rcpf(dn[0] + EPS);
      st128_dev(rw, lane * 16u, WS_O + (unsigned)((mt * 8 + wr * 4 + st) * 32 + (hq >> 1)) * 1024u, pack8(o0 * inv, o1 * inv));
    }
    wait_vm<0>();
    if (lane == 0) st64_dev(rw, WS_F3 + (unsigned)(((mt * 2 + wr) * 16 + hp) * 2 + wc) * FS, 0, (u32x2){tlo, thi});
    STAMP(6);
    BAR();
    STAMP(7);
    poll_flags(rw, WS_F3 + (unsigned)((mt2 * 2 + wr) * 32 + (lane & 31)) * FS, tlo, thi);
    if (b == 0 && tid == 256) *(u64*)a.ws = tag;
    STAMP(8);
  }
  {
    u32x4 of[32][2];
    const unsigned ob = WS_O + (unsigned)((mt2 * 8 + lw * 2) * 32) * 1024u;
    auto ldo = [&](int i){ of[i][0] = ld128_dev(rw, lane * 16u, ob + i * 1024u); of[i][1] = ld128_dev(rw, lane * 16u, ob + 32768u + i * 1024u); };
    #pragma unroll
    for (int i = 0; i < CWIN; ++i) ldo(i);
    SCHED();
    f32x4 acc[2][4];
    #pragma unroll
    for (int i = 0; i < 4; ++i) { acc[0][i] = (f32x4){0, 0, 0, 0}; acc[1][i] = (f32x4){0, 0, 0, 0}; }
    unsigned aW[4][3];
    #pragma unroll
    for (int ct = 0; ct < 4; ++ct) {
      aW[ct][0] = (lds0 + (unsigned)(4 * g + q4) * 128u + ((unsigned)((2 * g + (q4 >> 1)) & 3) << 5) + 8u * p4) ^ (ct << 5);
      aW[ct][1] = aW[ct][0] + 61440u; aW[ct][2] = aW[ct][0] + 122880u;
      asm volatile("" : "+v"(aW[ct][0]), "+v"(aW[ct][1]), "+v"(aW[ct][2]));
    }
    u32x4 wf[3][4];
    auto ldw = [&](int i){
      const unsigned u = wo_unit(i), k = u / 61440u;
      #pragma unroll
      for (int ct = 0; ct < 4; ++ct) wf[i % 3][ct] = lds_tr128(aW[ct][k] + (u - k * 61440u), aW[ct][k] + (u - k * 61440u) + 2048u);
    };
    ldw(0); ldw(1);
    #pragma unroll
    for (int i = 0; i < 32; ++i) {
      SCHED();
      if (i + CWIN < 32) ldo(i + CWIN);
      if (i + 2 < 32) ldw(i + 2);
      #pragma unroll
      for (int ct = 0; ct < 4; ++ct) { acc[0][ct] = mfma16(wf[i % 3][ct], of[i][0], acc[0][ct]); acc[1][ct] = mfma16(wf[i % 3][ct], of[i][1], acc[1][ct]); }
      __builtin_amdgcn_sched_group_barrier(0x020, 2, 0);
      #pragma unroll
      for (int j = 0; j < 8; ++j) { __builtin_amdgcn_sched_group_barrier(0x008, 1, 0); __builtin_amdgcn_sched_group_barrier(0x100, 1, 0); }
    }
    SCHED();
    STAMP(9);
    BAR();
    #pragma unroll
    for (int h = 0; h < 2; ++h) {
      const int rt = lw * 2 + h;
      const float gt = lds_rf(lds0 + L_G + (unsigned)(rt * 16 + l16) * 4u), og = rbf(1.0f - gt);
      u32x2 res[4];
      #pragma unroll
      for (int ct = 0; ct < 4; ++ct) {
        const unsigned dr = 16 * ct + 4 * g + q4;
        const u32x2 bv = lds_tr64(lds0 + L_BT + dr * 256u + (((unsigned)rt ^ (dr & 7u)) << 5) + 8u * p4);
        const f32x4 av = acc[h][ct];
        res[ct] = (u32x2){pk_bf16(gt * av[0] + og * bflo(bv.x), gt * av[1] + og * bfhi(bv.x)), pk_bf16(gt * av[2] + og * bflo(bv.y), gt * av[3] + og * bfhi(bv.y))};
      }
      u16* op = a.out + (size_t)(mt2 * 128 + rt * 16 + l16) * 1024 + nt * 64 + 16 * (g & 1) + 8 * (g >> 1);
      *(u32x4*)op = widen16(res[0], res[1]);
      *(u32x4*)(op + 32) = widen16(res[2], res[3]);
    }
    STAMP(14);
    STAMP(10);
    STAMP_OUT(0, 1, 2, 3, 11, 4, 5, 6, 7, 8, 9, 14, 10);
  }
}
#if defined(__HIP_DEVICE_COMPILE__)
#pragma clang attribute pop
#endif

extern "C" void kernel_launch(void* const* d_in, const int* in_sizes, int n_in, void* d_out, int out_size, void* d_ws, size_t ws_size, hipStream_t stream){
  Args a;
  a.x = (const u16*)d_in[0]; a.w = (const u16*)d_in[1]; a.wo = (const u16*)d_in[2];
  a.lar = (const u16*)d_in[4]; a.aim = (const u16*)d_in[5]; a.cr = (const u16*)d_in[6]; a.ci = (const u16*)d_in[7];
  a.ldt = (const u16*)d_in[8]; a.dsk = (const u16*)d_in[9]; a.gw = (const u16*)d_in[10];
  a.out = (u16*)d_out; a.ws = (char*)d_ws;
  (void)hipFuncSetAttribute((const void*)fused_kernel, hipFuncAttributeMaxDynamicSharedMemorySize, LDS_BYTES);
  fused_kernel<<<256, 768, LDS_BYTES, stream>>>(a);
}
```
